```python
import torch

def _softmax(x, axis=-1):
    return torch.softmax(x, dim=axis)

def _gelu(x):
    return torch.nn.functional.gelu(x, approximate='tanh')

def _silu(x):
    return torch.nn.functional.silu(x)

def _sigmoid(x):
    return torch.sigmoid(x)

def _layernorm(x, axis=-1, eps=1e-05):
    m = x.mean(dim=axis, keepdim=True)
    v = x.var(dim=axis, keepdim=True, unbiased=False)
    return (x - m) / torch.sqrt(v + eps)

def _scatter_add(tgt, idx, src, axis=0):
    return tgt.clone().index_add_(axis, idx, src)

def _segment_sum(src, seg, n):
    out = torch.zeros((n,) + src.shape[1:], dtype=src.dtype)
    return out.index_add_(0, seg, src)

def _pad(x, pw):
    flat = tuple((v for pair in reversed(pw) for v in pair))
    return torch.nn.functional.pad(x, flat)
kernel_name = 'gather_scatter__molecular_angular_painn__graph'

def setup_inputs():

    _w = lambda a: (lambda t: t.to(torch.bfloat16) if t.is_floating_point() else t)(torch.as_tensor(a))
    N, deg, D = (12500, 32, 128)
    n_rbf = max(4, 4)
    E = N * deg
    cutoff = 5.0
    freqs = (torch.arange(1, n_rbf + 1) * torch.pi / cutoff).to(float)
    return {'sv': _w((lambda *s: torch.randn(*s, dtype=torch.bfloat16) * 0.4)(N, 4, D) * 0.3), 'pos': _w((lambda *s: torch.randn(*s, dtype=torch.bfloat16) * 3.2)(N, 3) * 1.2), 'src': _w((lambda lo, hi, *s: torch.randint(lo, hi, s))(0, N, E).to(torch.int64)), 'dst': _w((lambda lo, hi, *s: torch.randint(lo, hi, s))(0, N, E).to(torch.int64)), 'freqs': _w(freqs), 'w_phi1': _w((lambda *s: torch.randn(*s, dtype=torch.bfloat16) * 3.2)(D, D) / D ** 0.5), 'w_phi2': _w((lambda *s: torch.randn(*s, dtype=torch.bfloat16) * 3.2)(D, 3 * D) / D ** 0.5), 'w_rbf': _w((lambda *s: torch.randn(*s, dtype=torch.bfloat16) * 3.2)(n_rbf, 3 * D) / n_rbf ** 0.5)}

def reference(sv, pos, src, dst, freqs, w_phi1, w_phi2, w_rbf):
    N, _, D = sv.shape
    E = src.shape[0]
    PI, cutoff = (3.141592653589793, 5.0)
    s, v = (sv[:, 0, :].clone(), sv[:, 1:, :].clone())
    r_vec = pos[src] - pos[dst]
    d = torch.sqrt((r_vec * r_vec).sum(-1) + 1e-08)
    r_hat = r_vec / d[:, None]
    rbf = torch.sin(d[:, None] * freqs[None, :]) / d[:, None]
    fcut = 0.5 * (torch.cos(PI * d / cutoff) + 1.0)
    fcut = torch.where(d < cutoff, fcut, 0.0 * d)
    Wf = rbf @ w_rbf * fcut[:, None]
    phi = (_silu(s @ w_phi1) @ w_phi2)[src]
    m = phi * Wf
    m_s, m_vv, m_vr = (m[:, :D].clone(), m[:, D:2 * D].clone(), m[:, 2 * D:].clone())
    ds = _segment_sum(m_s, dst, N)
    dv_e = m_vv[:, None, :] * v[src] + m_vr[:, None, :] * r_hat[:, :, None]
    dv = _segment_sum(dv_e.reshape(E, 3 * D), dst, N).reshape(N, 3, D)
    return sv + torch.concatenate([ds[:, None, :], dv], axis=1)
```

```cpp
#include <hip/hip_runtime.h>
#include <hip/hip_bf16.h>
#include <stdint.h>

typedef unsigned short u16;
__device__ __forceinline__ float bf2f(u16 x) { return __uint_as_float(((unsigned)x) << 16); }
__device__ __forceinline__ u16 f2bf(float f) { __hip_bfloat16 b = __float2bfloat16(f); return *(u16*)&b; }

constexpr int D = 128;

__global__ void phi_kernel(const u16* sv, const u16* w1, const u16* w2, float* phi, float* acc, int N) {
    __shared__ float s[D], h[D];
    int n = blockIdx.x, t = threadIdx.x;
    s[t] = bf2f(sv[(size_t)n * 4 * D + t]);
    for (int q = 0; q < 4; ++q) acc[(size_t)n * 4 * D + q * D + t] = 0.f;
    __syncthreads();
    float a = 0.f;
    for (int k = 0; k < D; ++k) a += s[k] * bf2f(w1[k * D + t]);
    h[t] = a / (1.f + expf(-a));
    __syncthreads();
    for (int j = 0; j < 3; ++j) {
        float b = 0.f;
        for (int k = 0; k < D; ++k) b += h[k] * bf2f(w2[k * 3 * D + j * D + t]);
        phi[(size_t)n * 3 * D + j * D + t] = b;
    }
}

__global__ void edge_kernel(const u16* sv, const u16* pos, const int* src, const int* dst, const u16* freqs, const u16* wr,
                            const float* phi, float* acc, int E) {
    int e = blockIdx.x, t = threadIdx.x;
    int s = src[e], d_ = dst[e];
    float rx = bf2f(pos[s * 3 + 0]) - bf2f(pos[d_ * 3 + 0]);
    float ry = bf2f(pos[s * 3 + 1]) - bf2f(pos[d_ * 3 + 1]);
    float rz = bf2f(pos[s * 3 + 2]) - bf2f(pos[d_ * 3 + 2]);
    float d = sqrtf(rx * rx + ry * ry + rz * rz + 1e-8f);
    if (!(d < 5.0f)) return;
    float fc = 0.5f * (cosf(3.14159265358979f * d / 5.0f) + 1.0f);
    float W[3] = {0, 0, 0};
    for (int k = 0; k < 4; ++k) {
        float g = sinf(d * bf2f(freqs[k])) / d;
        for (int j = 0; j < 3; ++j) W[j] += g * bf2f(wr[k * 3 * D + j * D + t]);
    }
    float ms = phi[(size_t)s * 3 * D + t] * W[0] * fc;
    float mvv = phi[(size_t)s * 3 * D + D + t] * W[1] * fc;
    float mvr = phi[(size_t)s * 3 * D + 2 * D + t] * W[2] * fc;
    float rh[3] = {rx / d, ry / d, rz / d};
    atomicAdd(&acc[(size_t)d_ * 4 * D + t], ms);
    for (int q = 0; q < 3; ++q)
        atomicAdd(&acc[(size_t)d_ * 4 * D + (q + 1) * D + t], mvv * bf2f(sv[(size_t)s * 4 * D + (q + 1) * D + t]) + mvr * rh[q]);
}

__global__ void fin_kernel(const u16* sv, const float* acc, u16* out, int n) {
    int i = blockIdx.x * blockDim.x + threadIdx.x;
    if (i < n) out[i] = f2bf(bf2f(sv[i]) + acc[i]);
}

extern "C" void kernel_launch(void* const* d_in, const int* in_sizes, int n_in,
                              void* d_out, int out_size, void* d_ws, size_t ws_size,
                              hipStream_t stream) {
    const u16* sv = (const u16*)d_in[0];
    const u16* pos = (const u16*)d_in[1];
    const int* src = (const int*)d_in[2];
    const int* dst = (const int*)d_in[3];
    const u16* freqs = (const u16*)d_in[4];
    const u16* w1 = (const u16*)d_in[5];
    const u16* w2 = (const u16*)d_in[6];
    const u16* wr = (const u16*)d_in[7];
    int N = in_sizes[0] / (4 * D), E = in_sizes[2];
    float* phi = (float*)d_ws;
    float* acc = phi + (size_t)N * 3 * D;
    phi_kernel<<<N, D, 0, stream>>>(sv, w1, w2, phi, acc, N);
    edge_kernel<<<E, D, 0, stream>>>(sv, pos, src, dst, freqs, wr, phi, acc, E);
    fin_kernel<<<(out_size + 255) / 256, 256, 0, stream>>>(sv, acc, (u16*)d_out, out_size);
}
```

```cpp
#include <hip/hip_runtime.h>
#include <stdint.h>

typedef unsigned short u16;
typedef unsigned int u32;
typedef short s4 __attribute__((ext_vector_type(4)));
typedef short s8 __attribute__((ext_vector_type(8)));
typedef __bf16 bf2 __attribute__((ext_vector_type(2)));
typedef __bf16 bf8 __attribute__((ext_vector_type(8)));
typedef float f2 __attribute__((ext_vector_type(2)));
typedef float f4 __attribute__((ext_vector_type(4)));
typedef float f16v __attribute__((ext_vector_type(16)));
typedef u32 u2 __attribute__((ext_vector_type(2)));
typedef u32 u4 __attribute__((ext_vector_type(4)));
typedef __attribute__((address_space(3))) s4* lds_s4p;
typedef __attribute__((address_space(3))) void* lds_vp;

constexpr int ROWB = 1280;
constexpr int CAP = 64;
constexpr int TM = 64;
constexpr int K1T = 512;
constexpr int NFILT = 60;
constexpr int EPT = 14;
constexpr int LDS_BYTES = 163840;
constexpr int OFF_W2 = 0, OFF_W1 = 98304, OFF_S = 131072, OFF_H = 147456;
constexpr int PPITCH = 1040;
constexpr int Q_OFF = 81920;
constexpr float CUTOFF = 5.0f;

__device__ __forceinline__ float lo2f(u32 x) { return __uint_as_float(x << 16); }
__device__ __forceinline__ float hi2f(u32 x) { return __uint_as_float(x & 0xffff0000u); }
__device__ __forceinline__ u32 pack2(float a, float b) {
    bf2 r = {(__bf16)a, (__bf16)b};
    return __builtin_bit_cast(u32, r);
}
__device__ __forceinline__ void dma16(const void* g, void* l) {
    __builtin_amdgcn_global_load_lds((const u32*)g, (u32*)l, 16, 0, 0);
}
__device__ __forceinline__ s4 tr_read(const char* p) {
    return __builtin_amdgcn_ds_read_tr16_b64_v4i16((lds_s4p)(lds_vp)(p));
}
__device__ __forceinline__ bf8 cat8(s4 a, s4 b) {
    s8 r = __builtin_shufflevector(a, b, 0, 1, 2, 3, 4, 5, 6, 7);
    return __builtin_bit_cast(bf8, r);
}

__device__ __forceinline__ void gemm_role(char* lds, int tile, const u16* __restrict__ sv, const u16* __restrict__ w1,
                                          const u16* __restrict__ w2, char* __restrict__ table, int N) {
    const int tid = threadIdx.x, lane = tid & 63, w = __builtin_amdgcn_readfirstlane(tid >> 6);
    const int n0 = tile * TM;
#pragma unroll
    for (int i = 0; i < 2; ++i) {
        int q = (2 * w + i) * 64 + lane, row = q >> 4, c = (q & 15) ^ (row & 15);
        int node = min(n0 + row, N - 1);
        dma16((const char*)sv + (size_t)node * 1024 + c * 16, lds + OFF_S + (2 * w + i) * 1024);
    }
    u32 vreg[8][3];
#pragma unroll
    for (int i = 0; i < 8; ++i) {
        int node = min(n0 + 8 * w + i, N - 1);
        const u32* p = (const u32*)((const char*)sv + (size_t)node * 1024 + 256) + lane;
#pragma unroll
        for (int q = 0; q < 3; ++q) vreg[i][q] = __builtin_nontemporal_load(p + 64 * q);
    }
    __builtin_amdgcn_sched_barrier(0);
#pragma unroll
    for (int i = 0; i < 4; ++i) {
        int q = (4 * w + i) * 64 + lane, k = q >> 4, cp = q & 15;
        int c = (((cp >> 2) ^ (k & 3)) << 2) | (cp & 3);
        dma16((const char*)w1 + k * 256 + c * 16, lds + OFF_W1 + (4 * w + i) * 1024);
    }
#pragma unroll
    for (int i = 0; i < 12; ++i) {
        int ql = i * 64 + lane, kl = ql / 48, cp = ql - kl * 48, k = 16 * w + kl;
        int c = (((cp >> 2) ^ (k & 3)) << 2) | (cp & 3);
        dma16((const char*)w2 + k * 768 + c * 16, lds + OFF_W2 + (12 * w + i) * 1024);
    }
    __builtin_amdgcn_sched_barrier(0);
    asm volatile("s_waitcnt vmcnt(12)" ::: "memory");
    __builtin_amdgcn_s_barrier();
    __builtin_amdgcn_sched_barrier(0);

    const int r = lane & 31, h = lane >> 5, g = lane >> 4, tq = (lane & 15) >> 2, tp = lane & 3;
    const int nt = w & 1;
    const int row = 32 * nt + r;
    {
        const int ht = w >> 1;
        const char* abase = lds + OFF_W1 + (8 * (g >> 1) + tq) * 256 + ((ht ^ tq) << 6) + 32 * (g & 1) + 8 * tp;
        const char* bbase = lds + OFF_S + row * 256;
        const int x = (h ^ (r & 15)) << 4;
        f16v acc = {};
#pragma unroll
        for (int ks = 0; ks < 8; ++ks) {
            s4 a0 = tr_read(abase + ks * 4096), a1 = tr_read(abase + ks * 4096 + 1024);
            bf8 b = *(const bf8*)(bbase + ((ks * 32) ^ x));
            acc = __builtin_amdgcn_mfma_f32_32x32x16_bf16(cat8(a0, a1), b, acc, 0, 0, 0);
        }
        char* hbase = lds + OFF_H + row * 256 + 8 * h;
#pragma unroll
        for (int gg = 0; gg < 4; ++gg) {
            float y[4];
#pragma unroll
            for (int j = 0; j < 4; ++j) { float a = acc[4 * gg + j]; y[j] = a / (1.0f + __expf(-a)); }
            u2 o = {pack2(y[0], y[1]), pack2(y[2], y[3])};
            *(u2*)(hbase + (((4 * ht + gg) ^ (r & 15)) << 4)) = o;
        }
    }
    asm volatile("s_waitcnt vmcnt(0) lgkmcnt(0)" ::: "memory");
    __builtin_amdgcn_s_barrier();
    __builtin_amdgcn_sched_barrier(0);
    f16v acc2[3] = {};
    const int ot0 = 3 * (w >> 1);
    {
        const char* bbase = lds + OFF_H + row * 256;
        const int x = (h ^ (r & 15)) << 4;
        const char* ab[3];
#pragma unroll
        for (int t = 0; t < 3; ++t)
            ab[t] = lds + OFF_W2 + (8 * (g >> 1) + tq) * 768 + (((ot0 + t) ^ tq) << 6) + 32 * (g & 1) + 8 * tp;
#pragma unroll
        for (int ks = 0; ks < 8; ++ks) {
            bf8 b = *(const bf8*)(bbase + ((ks * 32) ^ x));
#pragma unroll
            for (int t = 0; t < 3; ++t) {
                s4 a0 = tr_read(ab[t] + ks * 12288), a1 = tr_read(ab[t] + ks * 12288 + 3072);
                acc2[t] = __builtin_amdgcn_mfma_f32_32x32x16_bf16(cat8(a0, a1), b, acc2[t], 0, 0, 0);
            }
        }
    }
    __syncthreads();
    {
        char* prow = lds + row * PPITCH;
#pragma unroll
        for (int t = 0; t < 3; ++t) {
            const int ot = ot0 + t, seg = ot >> 2, cb = 32 * (ot & 3);
            if (seg == 1) {
#pragma unroll
                for (int gg = 0; gg < 4; ++gg) {
                    f4 o = {acc2[t][4 * gg], acc2[t][4 * gg + 1], acc2[t][4 * gg + 2], acc2[t][4 * gg + 3]};
                    *(f4*)(prow + 512 + (cb + 8 * gg + 4 * h) * 4) = o;
                }
            } else {
                char* pp = prow + (seg == 0 ? 0 : 256);
#pragma unroll
                for (int gg = 0; gg < 4; ++gg) {
                    u2 o = {pack2(acc2[t][4 * gg], acc2[t][4 * gg + 1]), pack2(acc2[t][4 * gg + 2], acc2[t][4 * gg + 3])};
                    *(u2*)(pp + (cb + 8 * gg + 4 * h) * 2) = o;
                }
            }
        }
    }
    __syncthreads();
#pragma unroll
    for (int i = 0; i < 8; ++i) {
        const int rr = 8 * w + i, node = n0 + rr;
        const char* prow = lds + rr * PPITCH;
        u32 s = *(const u32*)(prow + 4 * lane);
        u32 vr = *(const u32*)(prow + 256 + 4 * lane);
        f2 vv = *(const f2*)(prow + 512 + 8 * lane);
        u32 x = pack2(vv[0] * lo2f(vreg[i][0]), vv[1] * hi2f(vreg[i][0]));
        u32 y = pack2(vv[0] * lo2f(vreg[i][1]), vv[1] * hi2f(vreg[i][1]));
        u32 z = pack2(vv[0] * lo2f(vreg[i][2]), vv[1] * hi2f(vreg[i][2]));
        if (node < N) {
            char* trow = table + (size_t)node * ROWB;
            u4 o = {s, vr, x, y};
            *(u4*)(trow + 16 * lane) = o;
            *(u32*)(trow + 1024 + 4 * lane) = z;
        }
    }
}

template <bool POS_LDS>
__device__ __forceinline__ void get_pos(const char* lds, const u16* __restrict__ pos, int n, float& x, float& y, float& z) {
    if (POS_LDS) {
        const u32* p = (const u32*)lds + ((3 * n) >> 1);
        u32 d0 = p[0], d1 = p[1];
        u32 sh = (n & 1) << 4;
        u32 xy = __builtin_amdgcn_alignbit(d1, d0, sh);
        u32 zz = d1 >> sh;
        x = lo2f(xy); y = hi2f(xy); z = lo2f(zz);
    } else {
        x = __uint_as_float((u32)pos[3 * n] << 16);
        y = __uint_as_float((u32)pos[3 * n + 1] << 16);
        z = __uint_as_float((u32)pos[3 * n + 2] << 16);
    }
}

__device__ __forceinline__ u32 bf_hi(float x) { return pack2(x, 0.f) & 0xffffu; }

template <bool POS_LDS>
__device__ __forceinline__ void filter_role(char* lds, int f, const u16* __restrict__ pos, const int* __restrict__ src,
                                            const int* __restrict__ dst, const u16* __restrict__ freqs, int* __restrict__ cnt,
                                            u4* __restrict__ bucket, u4* __restrict__ ovf_rec, int* __restrict__ ovf_dst, int N, int E) {
    const int tid = threadIdx.x, lane = tid & 63, w = __builtin_amdgcn_readfirstlane(tid >> 6);
    const int chunk = (E + NFILT - 1) / NFILT;
    const int e0 = f * chunk, e1 = min(E, e0 + chunk);
    u32* qcnt = (u32*)(lds + Q_OFF);
    u2* queue = (u2*)(lds + Q_OFF + 16);
    if (POS_LDS) {
        const int bytes = N * 6, nch = bytes >> 4;
        for (int p = w; p * 64 < nch; p += 8) {
            int q = p * 64 + lane;
            if (q < nch) dma16((const char*)pos + q * 16, lds + p * 1024);
        }
        const int rem = (bytes - (nch << 4)) >> 1;
        if (tid < rem) ((u16*)lds)[nch * 8 + tid] = pos[nch * 8 + tid];
    }
    float fr[4];
#pragma unroll
    for (int k = 0; k < 4; ++k) fr[k] = __uint_as_float((u32)freqs[k] << 16);
    for (int base = e0; base < e1; base += K1T * EPT) {
        int es[EPT], ed[EPT];
#pragma unroll
        for (int i = 0; i < EPT; ++i) {
            int e = base + i * K1T + tid;
            bool ok = e < e1;
            es[i] = ok ? __builtin_nontemporal_load(src + e) : -1;
            ed[i] = ok ? __builtin_nontemporal_load(dst + e) : 0;
        }
        if (tid == 0) *qcnt = 0;
        asm volatile("s_waitcnt vmcnt(0) lgkmcnt(0)" ::: "memory");
        __syncthreads();
#pragma unroll
        for (int i = 0; i < EPT; ++i) {
            bool act = false;
            if (es[i] >= 0) {
                float ax, ay, az, bx, by, bz;
                get_pos<POS_LDS>(lds, pos, es[i], ax, ay, az);
                get_pos<POS_LDS>(lds, pos, ed[i], bx, by, bz);
                float rx = ax - bx, ry = ay - by, rz = az - bz;
                float d = sqrtf(rx * rx + ry * ry + rz * rz + 1e-8f);
                act = !(d >= CUTOFF && d <= 3.0e38f);
            }
            unsigned long long m = __ballot(act);
            if (m) {
                u32 b = 0;
                if (lane == 0) b = atomicAdd(qcnt, (u32)__popcll(m));
                b = __builtin_amdgcn_readfirstlane(b);
                if (act) {
                    u32 slot = b + __builtin_amdgcn_mbcnt_hi((u32)(m >> 32), __builtin_amdgcn_mbcnt_lo((u32)m, 0));
                    u2 o = {(u32)es[i], (u32)ed[i]};
                    queue[slot] = o;
                }
            }
        }
        __syncthreads();
        const int qn = *qcnt;
        for (int j = tid; j < qn; j += K1T) {
            u2 q = queue[j];
            const int s = q[0], dd = q[1];
            int slot = atomicAdd(&cnt[dd], 1);
            float ax, ay, az, bx, by, bz;
            get_pos<POS_LDS>(lds, pos, s, ax, ay, az);
            get_pos<POS_LDS>(lds, pos, dd, bx, by, bz);
            float rx = ax - bx, ry = ay - by, rz = az - bz;
            float d = sqrtf(rx * rx + ry * ry + rz * rz + 1e-8f);
            float inv = 1.0f / d;
            float fc = (d < CUTOFF) ? 0.5f * (cosf(3.14159265358979323846f * d / CUTOFF) + 1.0f) : 0.0f * d;
            float gk[4]; u32 hi[4], lo[4];
#pragma unroll
            for (int k = 0; k < 4; ++k) {
                gk[k] = sinf(d * fr[k]) * inv * fc;
                hi[k] = bf_hi(gk[k]);
                lo[k] = bf_hi(gk[k] - __uint_as_float(hi[k] << 16));
            }
            u4 ra = {hi[0] | (hi[1] << 16), hi[2] | (hi[3] << 16), lo[0] | (lo[1] << 16), lo[2] | (lo[3] << 16)};
            u4 rb = {(u32)s, __float_as_uint(rx * inv), __float_as_uint(ry * inv), __float_as_uint(rz * inv)};
            if (slot < CAP) {
                u4* p = bucket + ((size_t)dd * CAP + slot) * 2;
                p[0] = ra; p[1] = rb;
            } else {
                int o = atomicAdd(&cnt[N], 1);
                ovf_rec[(size_t)o * 2] = ra; ovf_rec[(size_t)o * 2 + 1] = rb; ovf_dst[o] = dd;
            }
        }
        __syncthreads();
    }
}

__global__ __launch_bounds__(K1T) void k1_kernel(const u16* __restrict__ sv, const u16* __restrict__ pos, const int* __restrict__ src,
                                                 const int* __restrict__ dst, const u16* __restrict__ freqs, const u16* __restrict__ w1,
                                                 const u16* __restrict__ w2, char* __restrict__ table, int* __restrict__ cnt,
                                                 u4* __restrict__ bucket, u4* __restrict__ ovf_rec, int* __restrict__ ovf_dst, int N, int E, int boff) {
    __shared__ __attribute__((aligned(1024))) char lds[LDS_BYTES];
    const int b = blockIdx.x + boff;
    if (b < NFILT) {
        if (N * 6 + 16 <= Q_OFF) filter_role<true>(lds, b, pos, src, dst, freqs, cnt, bucket, ovf_rec, ovf_dst, N, E);
        else filter_role<false>(lds, b, pos, src, dst, freqs, cnt, bucket, ovf_rec, ovf_dst, N, E);
    } else {
        gemm_role(lds, b - NFILT, sv, w1, w2, table, N);
    }
}

struct Acc { float s0, s1, x0, x1, y0, y1, z0, z1; };

__global__ __launch_bounds__(256) void k2_kernel(const u16* __restrict__ sv, const u16* __restrict__ wr, const int* __restrict__ cnt,
                                                 const u4* __restrict__ bucket, const u4* __restrict__ ovf_rec, const int* __restrict__ ovf_dst,
                                                 const char* __restrict__ table, u16* __restrict__ out, int N) {
    __shared__ u4 scratch[4][136];
    const int lane = threadIdx.x & 63, w = __builtin_amdgcn_readfirstlane(threadIdx.x >> 6);
    const int n = blockIdx.x * 4 + w;
    if (n >= N) return;
    u4* sc = scratch[w];
    const int ctot = cnt[n];
    const int c = min(ctot, CAP);
    u4 ra = {0, 0, 0, 0}, rb = {0xffffffffu, 0, 0, 0};
    if (lane < c) {
        const u4* p = bucket + ((size_t)n * CAP + lane) * 2;
        ra = p[0]; rb = p[1];
    }
    u32 res[4];
#pragma unroll
    for (int q = 0; q < 4; ++q) res[q] = *((const u32*)((const char*)sv + (size_t)n * 1024 + q * 256) + lane);
    s4 wb[3][2];
#pragma unroll
    for (int t = 0; t < 3; ++t) {
        u32 wk[4];
#pragma unroll
        for (int k = 0; k < 4; ++k) wk[k] = *((const u32*)(wr + k * 384 + t * 128) + lane);
        u2 b0 = {(wk[0] & 0xffffu) | (wk[1] << 16), (wk[2] & 0xffffu) | (wk[3] << 16)};
        u2 b1 = {(wk[0] >> 16) | (wk[1] & 0xffff0000u), (wk[2] >> 16) | (wk[3] & 0xffff0000u)};
        wb[t][0] = __builtin_bit_cast(s4, b0);
        wb[t][1] = __builtin_bit_cast(s4, b1);
    }
    auto rsrc = __builtin_amdgcn_make_buffer_rsrc((void*)table, 0, (u32)((size_t)N * ROWB), 0x00020000);
    float as0 = 0, as1 = 0, ax0 = 0, ax1 = 0, ay0 = 0, ay1 = 0, az0 = 0, az1 = 0;

    auto process = [&](int m) {
        for (int b = 0; b < m; b += 4) {
            u4 ga = sc[2 * (b + (lane & 3))];
            u4 meta[4]; u4 d4[4]; u32 d1[4];
#pragma unroll
            for (int i = 0; i < 4; ++i) {
                meta[i] = sc[2 * (b + i) + 1];
                if (b + i < m) {
                    u32 so = __builtin_amdgcn_readfirstlane(meta[i][0]) * ROWB;
                    d4[i] = __builtin_amdgcn_raw_buffer_load_b128(rsrc, lane * 16, so, 0);
                    d1[i] = __builtin_amdgcn_raw_buffer_load_b32(rsrc, 1024 + lane * 4, so, 0);
                }
            }
            u2 ghi = {ga[0], ga[1]}, glo = {ga[2], ga[3]};
            f4 W[3][2];
#pragma unroll
            for (int t = 0; t < 3; ++t)
#pragma unroll
                for (int cc = 0; cc < 2; ++cc) {
                    f4 z = {0, 0, 0, 0};
                    z = __builtin_amdgcn_mfma_f32_4x4x4bf16_1k(__builtin_bit_cast(s4, glo), wb[t][cc], z, 0, 0, 0);
                    W[t][cc] = __builtin_amdgcn_mfma_f32_4x4x4bf16_1k(__builtin_bit_cast(s4, ghi), wb[t][cc], z, 0, 0, 0);
                }
#pragma unroll
            for (int i = 0; i < 4; ++i) {
                if (b + i < m) {
                    float rx = __uint_as_float(meta[i][1]), ry = __uint_as_float(meta[i][2]), rz = __uint_as_float(meta[i][3]);
                    as0 += W[0][0][i] * lo2f(d4[i][0]); as1 += W[0][1][i] * hi2f(d4[i][0]);
                    float t0 = W[2][0][i] * lo2f(d4[i][1]), t1 = W[2][1][i] * hi2f(d4[i][1]);
                    ax0 += W[1][0][i] * lo2f(d4[i][2]) + t0 * rx; ax1 += W[1][1][i] * hi2f(d4[i][2]) + t1 * rx;
                    ay0 += W[1][0][i] * lo2f(d4[i][3]) + t0 * ry; ay1 += W[1][1][i] * hi2f(d4[i][3]) + t1 * ry;
                    az0 += W[1][0][i] * lo2f(d1[i]) + t0 * rz;    az1 += W[1][1][i] * hi2f(d1[i]) + t1 * rz;
                }
            }
        }
    };

    if (c > 0) {
        int rank = 0;
        const u32 key = rb[0];
        for (int j = 0; j < c; ++j) {
            u32 kj = __builtin_amdgcn_readlane(key, j);
            rank += (kj < key || (kj == key && j < lane)) ? 1 : 0;
        }
        if (lane >= c) rank = lane;
        sc[2 * rank] = ra; sc[2 * rank + 1] = rb;
        if (lane < 4) { u4 z = {0, 0, 0, 0}; sc[2 * (64 + lane)] = z; sc[2 * (64 + lane) + 1] = z; }
        __builtin_amdgcn_wave_barrier();
        asm volatile("s_waitcnt lgkmcnt(0)" ::: "memory");
        process(c);
    }
    if (ctot > CAP) {
        const int novf = cnt[N];
        for (int base = 0; base < novf; base += 64) {
            int j = base + lane;
            bool mt = (j < novf) && (ovf_dst[j] == n);
            unsigned long long m = __ballot(mt);
            if (m) {
                int k = __popcll(m);
                int slot = __builtin_amdgcn_mbcnt_hi((u32)(m >> 32), __builtin_amdgcn_mbcnt_lo((u32)m, 0));
                u4 z = {0, 0, 0, 0};
                __builtin_amdgcn_wave_barrier();
                sc[2 * lane] = z; sc[2 * lane + 1] = z;
                if (lane < 4) { sc[2 * (64 + lane)] = z; sc[2 * (64 + lane) + 1] = z; }
                __builtin_amdgcn_wave_barrier();
                asm volatile("s_waitcnt lgkmcnt(0)" ::: "memory");
                if (mt) { sc[2 * slot] = ovf_rec[(size_t)j * 2]; sc[2 * slot + 1] = ovf_rec[(size_t)j * 2 + 1]; }
                __builtin_amdgcn_wave_barrier();
                asm volatile("s_waitcnt lgkmcnt(0)" ::: "memory");
                process(k);
            }
        }
    }
    u32* o = (u32*)((char*)out + (size_t)n * 1024) + lane;
    o[0] = pack2(lo2f(res[0]) + as0, hi2f(res[0]) + as1);
    o[64] = pack2(lo2f(res[1]) + ax0, hi2f(res[1]) + ax1);
    o[128] = pack2(lo2f(res[2]) + ay0, hi2f(res[2]) + ay1);
    o[192] = pack2(lo2f(res[3]) + az0, hi2f(res[3]) + az1);
}

extern "C" void kernel_launch(void* const* d_in, const int* in_sizes, int n_in, void* d_out, int out_size, void* d_ws, size_t ws_size,
                              hipStream_t stream) {
    const u16* sv = (const u16*)d_in[0];
    const u16* pos = (const u16*)d_in[1];
    const int* src = (const int*)d_in[2];
    const int* dst = (const int*)d_in[3];
    const u16* freqs = (const u16*)d_in[4];
    const u16* w1 = (const u16*)d_in[5];
    const u16* w2 = (const u16*)d_in[6];
    const u16* wr = (const u16*)d_in[7];
    const int N = in_sizes[0] / 512, E = in_sizes[2];
    char* ws = (char*)d_ws;
    size_t off = 0;
    int* cnt = (int*)ws; size_t cnt_bytes = (((size_t)(N + 1) * 4 + 15) / 16) * 16; off = (cnt_bytes + 255) / 256 * 256;
    char* table = ws + off; off += ((size_t)N * ROWB + 255) / 256 * 256;
    u4* bucket = (u4*)(ws + off); off += (size_t)N * CAP * 32;
    u4* ovf_rec = (u4*)(ws + off); off += (size_t)E * 32;
    int* ovf_dst = (int*)(ws + off);
    hipMemsetAsync(cnt, 0, cnt_bytes, stream);
    const int ntiles = (N + TM - 1) / TM;
    k1_kernel<<<NFILT + ntiles, K1T, 0, stream>>>(sv, pos, src, dst, freqs, w1, w2, table, cnt, bucket, ovf_rec, ovf_dst, N, E, 0);
    k2_kernel<<<(N + 3) / 4, 256, 0, stream>>>(sv, wr, cnt, bucket, ovf_rec, ovf_dst, table, (u16*)d_out, N);
}
```

```cpp
#include <hip/hip_runtime.h>
#include <stdint.h>

typedef unsigned short u16;
typedef unsigned int u32;
typedef short s4 __attribute__((ext_vector_type(4)));
typedef short s8 __attribute__((ext_vector_type(8)));
typedef __bf16 bf2 __attribute__((ext_vector_type(2)));
typedef __bf16 bf8 __attribute__((ext_vector_type(8)));
typedef float f2 __attribute__((ext_vector_type(2)));
typedef float f4 __attribute__((ext_vector_type(4)));
typedef float f16v __attribute__((ext_vector_type(16)));
typedef u32 u2 __attribute__((ext_vector_type(2)));
typedef u32 u4 __attribute__((ext_vector_type(4)));
typedef __attribute__((address_space(3))) s4* lds_s4p;
typedef __attribute__((address_space(3))) void* lds_vp;

constexpr int ROWB = 1280;
constexpr int CAP = 64;
constexpr int TM = 64;
constexpr int K1T = 512;
constexpr int NFILT = 60;
constexpr int EPT = 14;
constexpr int LDS_BYTES = 163840;
constexpr int OFF_W2 = 0, OFF_W1 = 98304, OFF_S = 131072, OFF_H = 147456;
constexpr int PPITCH = 1040;
constexpr int Q_OFF = 81920;
constexpr float CUTOFF = 5.0f;

__device__ __forceinline__ float lo2f(u32 x) { return __uint_as_float(x << 16); }
__device__ __forceinline__ float hi2f(u32 x) { return __uint_as_float(x & 0xffff0000u); }
__device__ __forceinline__ u32 pack2(float a, float b) {
    bf2 r = {(__bf16)a, (__bf16)b};
    return __builtin_bit_cast(u32, r);
}
__device__ __forceinline__ void dma16(const void* g, void* l) {
    __builtin_amdgcn_global_load_lds((const u32*)g, (u32*)l, 16, 0, 0);
}
__device__ __forceinline__ s4 tr_read(const char* p) {
    return __builtin_amdgcn_ds_read_tr16_b64_v4i16((lds_s4p)(lds_vp)(p));
}
__device__ __forceinline__ bf8 cat8(s4 a, s4 b) {
    s8 r = __builtin_shufflevector(a, b, 0, 1, 2, 3, 4, 5, 6, 7);
    return __builtin_bit_cast(bf8, r);
}

#define STAMP(idx)
__device__ __forceinline__ void gemm_role(char* lds, int tile, const u16* __restrict__ sv, const u16* __restrict__ w1,
                                          const u16* __restrict__ w2, char* __restrict__ table, int N) {
    const int tid = threadIdx.x, lane = tid & 63, w = __builtin_amdgcn_readfirstlane(tid >> 6);
    const int n0 = tile * TM;
    const int stamp_base = 0;
    STAMP(0);
#pragma unroll
    for (int i = 0; i < 2; ++i) {
        int q = (2 * w + i) * 64 + lane, row = q >> 4, c = (q & 15) ^ (row & 15);
        int node = min(n0 + row, N - 1);
        dma16((const char*)sv + (size_t)node * 1024 + c * 16, lds + OFF_S + (2 * w + i) * 1024);
    }
    u32 vreg[8][3];
#pragma unroll
    for (int i = 0; i < 8; ++i) {
        int node = min(n0 + 8 * w + i, N - 1);
        const u32* p = (const u32*)((const char*)sv + (size_t)node * 1024 + 256) + lane;
#pragma unroll
        for (int q = 0; q < 3; ++q) vreg[i][q] = __builtin_nontemporal_load(p + 64 * q);
    }
    __builtin_amdgcn_sched_barrier(0);
#pragma unroll
    for (int i = 0; i < 4; ++i) {
        int q = (4 * w + i) * 64 + lane, k = q >> 4, cp = q & 15;
        int c = (((cp >> 2) ^ (k & 3)) << 2) | (cp & 3);
        dma16((const char*)w1 + k * 256 + c * 16, lds + OFF_W1 + (4 * w + i) * 1024);
    }
#pragma unroll
    for (int i = 0; i < 12; ++i) {
        int ql = i * 64 + lane, kl = ql / 48, cp = ql - kl * 48, k = 16 * w + kl;
        int c = (((cp >> 2) ^ (k & 3)) << 2) | (cp & 3);
        dma16((const char*)w2 + k * 768 + c * 16, lds + OFF_W2 + (12 * w + i) * 1024);
    }
    __builtin_amdgcn_sched_barrier(0);
    asm volatile("s_waitcnt vmcnt(12)" ::: "memory");
    __builtin_amdgcn_s_barrier();
    __builtin_amdgcn_sched_barrier(0);
    STAMP(1);

    const int r = lane & 31, h = lane >> 5, g = lane >> 4, tq = (lane & 15) >> 2, tp = lane & 3;
    const int nt = w & 1;
    const int row = 32 * nt + r;
    {
        const int ht = w >> 1;
        const char* abase = lds + OFF_W1 + (8 * (g >> 1) + tq) * 256 + ((ht ^ tq) << 6) + 32 * (g & 1) + 8 * tp;
        const char* bbase = lds + OFF_S + row * 256;
        const int x = (h ^ (r & 15)) << 4;
        f16v acc = {};
#pragma unroll
        for (int ks = 0; ks < 8; ++ks) {
            s4 a0 = tr_read(abase + ks * 4096), a1 = tr_read(abase + ks * 4096 + 1024);
            bf8 b = *(const bf8*)(bbase + ((ks * 32) ^ x));
            acc = __builtin_amdgcn_mfma_f32_32x32x16_bf16(cat8(a0, a1), b, acc, 0, 0, 0);
        }
        char* hbase = lds + OFF_H + row * 256 + 8 * h;
#pragma unroll
        for (int gg = 0; gg < 4; ++gg) {
            float y[4];
#pragma unroll
            for (int j = 0; j < 4; ++j) { float a = acc[4 * gg + j]; y[j] = a / (1.0f + __expf(-a)); }
            u2 o = {pack2(y[0], y[1]), pack2(y[2], y[3])};
            *(u2*)(hbase + (((4 * ht + gg) ^ (r & 15)) << 4)) = o;
        }
    }
    STAMP(2);
    asm volatile("s_waitcnt vmcnt(0) lgkmcnt(0)" ::: "memory");
    __builtin_amdgcn_s_barrier();
    __builtin_amdgcn_sched_barrier(0);
    STAMP(3);
    f16v acc2[3] = {};
    const int ot0 = 3 * (w >> 1);
    {
        const char* bbase = lds + OFF_H + row * 256;
        const int x = (h ^ (r & 15)) << 4;
        const char* ab[3];
#pragma unroll
        for (int t = 0; t < 3; ++t)
            ab[t] = lds + OFF_W2 + (8 * (g >> 1) + tq) * 768 + (((ot0 + t) ^ tq) << 6) + 32 * (g & 1) + 8 * tp;
#pragma unroll
        for (int ks = 0; ks < 8; ++ks) {
            bf8 b = *(const bf8*)(bbase + ((ks * 32) ^ x));
#pragma unroll
            for (int t = 0; t < 3; ++t) {
                s4 a0 = tr_read(ab[t] + ks * 12288), a1 = tr_read(ab[t] + ks * 12288 + 3072);
                acc2[t] = __builtin_amdgcn_mfma_f32_32x32x16_bf16(cat8(a0, a1), b, acc2[t], 0, 0, 0);
            }
        }
    }
    __syncthreads();
    STAMP(4);
    {
        char* prow = lds + row * PPITCH;
#pragma unroll
        for (int t = 0; t < 3; ++t) {
            const int ot = ot0 + t, seg = ot >> 2, cb = 32 * (ot & 3);
            if (seg == 1) {
#pragma unroll
                for (int gg = 0; gg < 4; ++gg) {
                    f4 o = {acc2[t][4 * gg], acc2[t][4 * gg + 1], acc2[t][4 * gg + 2], acc2[t][4 * gg + 3]};
                    *(f4*)(prow + 512 + (cb + 8 * gg + 4 * h) * 4) = o;
                }
            } else {
                char* pp = prow + (seg == 0 ? 0 : 256);
#pragma unroll
                for (int gg = 0; gg < 4; ++gg) {
                    u2 o = {pack2(acc2[t][4 * gg], acc2[t][4 * gg + 1]), pack2(acc2[t][4 * gg + 2], acc2[t][4 * gg + 3])};
                    *(u2*)(pp + (cb + 8 * gg + 4 * h) * 2) = o;
                }
            }
        }
    }
    __syncthreads();
    STAMP(5);
#pragma unroll
    for (int i = 0; i < 8; ++i) {
        const int rr = 8 * w + i, node = n0 + rr;
        const char* prow = lds + rr * PPITCH;
        u32 s = *(const u32*)(prow + 4 * lane);
        u32 vr = *(const u32*)(prow + 256 + 4 * lane);
        f2 vv = *(const f2*)(prow + 512 + 8 * lane);
        u32 x = pack2(vv[0] * lo2f(vreg[i][0]), vv[1] * hi2f(vreg[i][0]));
        u32 y = pack2(vv[0] * lo2f(vreg[i][1]), vv[1] * hi2f(vreg[i][1]));
        u32 z = pack2(vv[0] * lo2f(vreg[i][2]), vv[1] * hi2f(vreg[i][2]));
        if (node < N) {
            char* trow = table + (size_t)node * ROWB;
            u4 o = {s, vr, x, y};
            *(u4*)(trow + 16 * lane) = o;
            *(u32*)(trow + 1024 + 4 * lane) = z;
        }
    }
    STAMP(6);
    asm volatile("s_waitcnt vmcnt(0)" ::: "memory");
    STAMP(7);
}

template <bool POS_LDS>
__device__ __forceinline__ void get_pos(const char* lds, const u16* __restrict__ pos, int n, float& x, float& y, float& z) {
    if (POS_LDS) {
        const u32* p = (const u32*)lds + ((3 * n) >> 1);
        u32 d0 = p[0], d1 = p[1];
        u32 sh = (n & 1) << 4;
        u32 xy = __builtin_amdgcn_alignbit(d1, d0, sh);
        u32 zz = d1 >> sh;
        x = lo2f(xy); y = hi2f(xy); z = lo2f(zz);
    } else {
        x = __uint_as_float((u32)pos[3 * n] << 16);
        y = __uint_as_float((u32)pos[3 * n + 1] << 16);
        z = __uint_as_float((u32)pos[3 * n + 2] << 16);
    }
}

__device__ __forceinline__ u32 bf_hi(float x) { return pack2(x, 0.f) & 0xffffu; }

template <bool POS_LDS>
__device__ __forceinline__ void filter_role(char* lds, int f, const u16* __restrict__ pos, const int* __restrict__ src,
                                            const int* __restrict__ dst, const u16* __restrict__ freqs, int* __restrict__ cnt,
                                            u4* __restrict__ bucket, u4* __restrict__ ovf_rec, int* __restrict__ ovf_dst, int N, int E) {
    const int tid = threadIdx.x, lane = tid & 63, w = __builtin_amdgcn_readfirstlane(tid >> 6);
    const int stamp_base = 0;
    STAMP(0);
    const int chunk = (E + NFILT - 1) / NFILT;
    const int e0 = f * chunk, e1 = min(E, e0 + chunk);
    u32* qcnt = (u32*)(lds + Q_OFF);
    u2* queue = (u2*)(lds + Q_OFF + 16);
    if (POS_LDS) {
        const int bytes = N * 6, nch = bytes >> 4;
        for (int p = w; p * 64 < nch; p += 8) {
            int q = p * 64 + lane;
            if (q < nch) dma16((const char*)pos + q * 16, lds + p * 1024);
        }
        const int rem = (bytes - (nch << 4)) >> 1;
        if (tid < rem) ((u16*)lds)[nch * 8 + tid] = pos[nch * 8 + tid];
    }
    float fr[4];
#pragma unroll
    for (int k = 0; k < 4; ++k) fr[k] = __uint_as_float((u32)freqs[k] << 16);
    for (int base = e0; base < e1; base += K1T * EPT) {
        int es[EPT], ed[EPT];
#pragma unroll
        for (int i = 0; i < EPT; ++i) {
            int e = base + i * K1T + tid;
            bool ok = e < e1;
            es[i] = ok ? __builtin_nontemporal_load(src + e) : -1;
            ed[i] = ok ? __builtin_nontemporal_load(dst + e) : 0;
        }
        if (tid == 0) *qcnt = 0;
        asm volatile("s_waitcnt vmcnt(0) lgkmcnt(0)" ::: "memory");
        __syncthreads();
        STAMP(1);
#pragma unroll
        for (int i = 0; i < EPT; ++i) {
            bool act = false;
            if (es[i] >= 0) {
                float ax, ay, az, bx, by, bz;
                get_pos<POS_LDS>(lds, pos, es[i], ax, ay, az);
                get_pos<POS_LDS>(lds, pos, ed[i], bx, by, bz);
                float rx = ax - bx, ry = ay - by, rz = az - bz;
                float d = sqrtf(rx * rx + ry * ry + rz * rz + 1e-8f);
                act = !(d >= CUTOFF && d <= 3.0e38f);
            }
            unsigned long long m = __ballot(act);
            if (m) {
                u32 b = 0;
                if (lane == 0) b = atomicAdd(qcnt, (u32)__popcll(m));
                b = __builtin_amdgcn_readfirstlane(b);
                if (act) {
                    u32 slot = b + __builtin_amdgcn_mbcnt_hi((u32)(m >> 32), __builtin_amdgcn_mbcnt_lo((u32)m, 0));
                    u2 o = {(u32)es[i], (u32)ed[i]};
                    queue[slot] = o;
                }
            }
        }
        __syncthreads();
        STAMP(2);
        const int qn = *qcnt;
        for (int j = tid; j < qn; j += K1T) {
            u2 q = queue[j];
            const int s = q[0], dd = q[1];
            int slot = atomicAdd(&cnt[dd], 1);
            float ax, ay, az, bx, by, bz;
            get_pos<POS_LDS>(lds, pos, s, ax, ay, az);
            get_pos<POS_LDS>(lds, pos, dd, bx, by, bz);
            float rx = ax - bx, ry = ay - by, rz = az - bz;
            float d = sqrtf(rx * rx + ry * ry + rz * rz + 1e-8f);
            float inv = 1.0f / d;
            float fc = (d < CUTOFF) ? 0.5f * (cosf(3.14159265358979323846f * d / CUTOFF) + 1.0f) : 0.0f * d;
            float gk[4]; u32 hi[4], lo[4];
#pragma unroll
            for (int k = 0; k < 4; ++k) {
                gk[k] = sinf(d * fr[k]) * inv * fc;
                hi[k] = bf_hi(gk[k]);
                lo[k] = bf_hi(gk[k] - __uint_as_float(hi[k] << 16));
            }
            u4 ra = {hi[0] | (hi[1] << 16), hi[2] | (hi[3] << 16), lo[0] | (lo[1] << 16), lo[2] | (lo[3] << 16)};
            u4 rb = {(u32)s, __float_as_uint(rx * inv), __float_as_uint(ry * inv), __float_as_uint(rz * inv)};
            if (slot < CAP) {
                u4* p = bucket + ((size_t)dd * CAP + slot) * 2;
                p[0] = ra; p[1] = rb;
            } else {
                int o = atomicAdd(&cnt[N], 1);
                ovf_rec[(size_t)o * 2] = ra; ovf_rec[(size_t)o * 2 + 1] = rb; ovf_dst[o] = dd;
            }
        }
        STAMP(3);
        asm volatile("s_waitcnt vmcnt(0)" ::: "memory");
        __syncthreads();
        STAMP(4);
    }
}

__global__ __launch_bounds__(K1T) void k1_kernel(const u16* __restrict__ sv, const u16* __restrict__ pos, const int* __restrict__ src,
                                                 const int* __restrict__ dst, const u16* __restrict__ freqs, const u16* __restrict__ w1,
                                                 const u16* __restrict__ w2, char* __restrict__ table, int* __restrict__ cnt,
                                                 u4* __restrict__ bucket, u4* __restrict__ ovf_rec, int* __restrict__ ovf_dst, int N, int E, int boff) {
    __shared__ __attribute__((aligned(1024))) char lds[LDS_BYTES];
    const int b = blockIdx.x + boff;
    if (b < NFILT) {
        if (N * 6 + 16 <= Q_OFF) filter_role<true>(lds, b, pos, src, dst, freqs, cnt, bucket, ovf_rec, ovf_dst, N, E);
        else filter_role<false>(lds, b, pos, src, dst, freqs, cnt, bucket, ovf_rec, ovf_dst, N, E);
    } else {
        gemm_role(lds, b - NFILT, sv, w1, w2, table, N);
    }
}

struct Acc { float s0, s1, x0, x1, y0, y1, z0, z1; };

__global__ __launch_bounds__(256) void k2_kernel(const u16* __restrict__ sv, const u16* __restrict__ wr, const int* __restrict__ cnt,
                                                 const u4* __restrict__ bucket, const u4* __restrict__ ovf_rec, const int* __restrict__ ovf_dst,
                                                 const char* __restrict__ table, u16* __restrict__ out, int N) {
    __shared__ u4 scratch[4][136];
    const int lane = threadIdx.x & 63, w = __builtin_amdgcn_readfirstlane(threadIdx.x >> 6);
    const int n = blockIdx.x * 4 + w;
    if (n >= N) return;
    const int stamp_base = 512;
    STAMP(0);
    u4* sc = scratch[w];
    const int ctot = cnt[n];
    const int c = min(ctot, CAP);
    u4 ra = {0, 0, 0, 0}, rb = {0xffffffffu, 0, 0, 0};
    if (lane < c) {
        const u4* p = bucket + ((size_t)n * CAP + lane) * 2;
        ra = p[0]; rb = p[1];
    }
    u32 res[4];
#pragma unroll
    for (int q = 0; q < 4; ++q) res[q] = *((const u32*)((const char*)sv + (size_t)n * 1024 + q * 256) + lane);
    s4 wb[3][2];
#pragma unroll
    for (int t = 0; t < 3; ++t) {
        u32 wk[4];
#pragma unroll
        for (int k = 0; k < 4; ++k) wk[k] = *((const u32*)(wr + k * 384 + t * 128) + lane);
        u2 b0 = {(wk[0] & 0xffffu) | (wk[1] << 16), (wk[2] & 0xffffu) | (wk[3] << 16)};
        u2 b1 = {(wk[0] >> 16) | (wk[1] & 0xffff0000u), (wk[2] >> 16) | (wk[3] & 0xffff0000u)};
        wb[t][0] = __builtin_bit_cast(s4, b0);
        wb[t][1] = __builtin_bit_cast(s4, b1);
    }
    auto rsrc = __builtin_amdgcn_make_buffer_rsrc((void*)table, 0, (u32)((size_t)N * ROWB), 0x00020000);
    float as0 = 0, as1 = 0, ax0 = 0, ax1 = 0, ay0 = 0, ay1 = 0, az0 = 0, az1 = 0;

    auto process = [&](int m) {
        for (int b = 0; b < m; b += 4) {
            u4 ga = sc[2 * (b + (lane & 3))];
            u4 meta[4]; u4 d4[4]; u32 d1[4];
#pragma unroll
            for (int i = 0; i < 4; ++i) {
                meta[i] = sc[2 * (b + i) + 1];
                if (b + i < m) {
                    u32 so = __builtin_amdgcn_readfirstlane(meta[i][0]) * ROWB;
                    d4[i] = __builtin_amdgcn_raw_buffer_load_b128(rsrc, lane * 16, so, 0);
                    d1[i] = __builtin_amdgcn_raw_buffer_load_b32(rsrc, 1024 + lane * 4, so, 0);
                }
            }
            u2 ghi = {ga[0], ga[1]}, glo = {ga[2], ga[3]};
            f4 W[3][2];
#pragma unroll
            for (int t = 0; t < 3; ++t)
#pragma unroll
                for (int cc = 0; cc < 2; ++cc) {
                    f4 z = {0, 0, 0, 0};
                    z = __builtin_amdgcn_mfma_f32_4x4x4bf16_1k(__builtin_bit_cast(s4, glo), wb[t][cc], z, 0, 0, 0);
                    W[t][cc] = __builtin_amdgcn_mfma_f32_4x4x4bf16_1k(__builtin_bit_cast(s4, ghi), wb[t][cc], z, 0, 0, 0);
                }
#pragma unroll
            for (int i = 0; i < 4; ++i) {
                if (b + i < m) {
                    float rx = __uint_as_float(meta[i][1]), ry = __uint_as_float(meta[i][2]), rz = __uint_as_float(meta[i][3]);
                    as0 += W[0][0][i] * lo2f(d4[i][0]); as1 += W[0][1][i] * hi2f(d4[i][0]);
                    float t0 = W[2][0][i] * lo2f(d4[i][1]), t1 = W[2][1][i] * hi2f(d4[i][1]);
                    ax0 += W[1][0][i] * lo2f(d4[i][2]) + t0 * rx; ax1 += W[1][1][i] * hi2f(d4[i][2]) + t1 * rx;
                    ay0 += W[1][0][i] * lo2f(d4[i][3]) + t0 * ry; ay1 += W[1][1][i] * hi2f(d4[i][3]) + t1 * ry;
                    az0 += W[1][0][i] * lo2f(d1[i]) + t0 * rz;    az1 += W[1][1][i] * hi2f(d1[i]) + t1 * rz;
                }
            }
        }
    };

    if (c > 0) {
        int rank = 0;
        const u32 key = rb[0];
        for (int j = 0; j < c; ++j) {
            u32 kj = __builtin_amdgcn_readlane(key, j);
            rank += (kj < key || (kj == key && j < lane)) ? 1 : 0;
        }
        if (lane >= c) rank = lane;
        sc[2 * rank] = ra; sc[2 * rank + 1] = rb;
        if (lane < 4) { u4 z = {0, 0, 0, 0}; sc[2 * (64 + lane)] = z; sc[2 * (64 + lane) + 1] = z; }
        __builtin_amdgcn_wave_barrier();
        asm volatile("s_waitcnt lgkmcnt(0)" ::: "memory");
        STAMP(1);
        process(c);
    }
    STAMP(2);
    if (ctot > CAP) {
        const int novf = cnt[N];
        for (int base = 0; base < novf; base += 64) {
            int j = base + lane;
            bool mt = (j < novf) && (ovf_dst[j] == n);
            unsigned long long m = __ballot(mt);
            if (m) {
                int k = __popcll(m);
                int slot = __builtin_amdgcn_mbcnt_hi((u32)(m >> 32), __builtin_amdgcn_mbcnt_lo((u32)m, 0));
                u4 z = {0, 0, 0, 0};
                __builtin_amdgcn_wave_barrier();
                sc[2 * lane] = z; sc[2 * lane + 1] = z;
                if (lane < 4) { sc[2 * (64 + lane)] = z; sc[2 * (64 + lane) + 1] = z; }
                __builtin_amdgcn_wave_barrier();
                asm volatile("s_waitcnt lgkmcnt(0)" ::: "memory");
                if (mt) { sc[2 * slot] = ovf_rec[(size_t)j * 2]; sc[2 * slot + 1] = ovf_rec[(size_t)j * 2 + 1]; }
                __builtin_amdgcn_wave_barrier();
                asm volatile("s_waitcnt lgkmcnt(0)" ::: "memory");
                process(k);
            }
        }
    }
    u32* o = (u32*)((char*)out + (size_t)n * 1024) + lane;
    o[0] = pack2(lo2f(res[0]) + as0, hi2f(res[0]) + as1);
    o[64] = pack2(lo2f(res[1]) + ax0, hi2f(res[1]) + ax1);
    o[128] = pack2(lo2f(res[2]) + ay0, hi2f(res[2]) + ay1);
    o[192] = pack2(lo2f(res[3]) + az0, hi2f(res[3]) + az1);
    STAMP(3);
}

extern "C" void kernel_launch(void* const* d_in, const int* in_sizes, int n_in, void* d_out, int out_size, void* d_ws, size_t ws_size,
                              hipStream_t stream) {
    const u16* sv = (const u16*)d_in[0];
    const u16* pos = (const u16*)d_in[1];
    const int* src = (const int*)d_in[2];
    const int* dst = (const int*)d_in[3];
    const u16* freqs = (const u16*)d_in[4];
    const u16* w1 = (const u16*)d_in[5];
    const u16* w2 = (const u16*)d_in[6];
    const u16* wr = (const u16*)d_in[7];
    const int N = in_sizes[0] / 512, E = in_sizes[2];
    char* ws = (char*)d_ws;
    size_t off = 0;
    int* cnt = (int*)ws; size_t cnt_bytes = (((size_t)(N + 1) * 4 + 15) / 16) * 16; off = (cnt_bytes + 255) / 256 * 256;
    char* table = ws + off; off += ((size_t)N * ROWB + 255) / 256 * 256;
    u4* bucket = (u4*)(ws + off); off += (size_t)N * CAP * 32;
    u4* ovf_rec = (u4*)(ws + off); off += (size_t)E * 32;
    int* ovf_dst = (int*)(ws + off);
    hipMemsetAsync(cnt, 0, cnt_bytes, stream);
    const int ntiles = (N + TM - 1) / TM;
    k1_kernel<<<NFILT + ntiles, K1T, 0, stream>>>(sv, pos, src, dst, freqs, w1, w2, table, cnt, bucket, ovf_rec, ovf_dst, N, E, 0);
    k2_kernel<<<(N + 3) / 4, 256, 0, stream>>>(sv, wr, cnt, bucket, ovf_rec, ovf_dst, table, (u16*)d_out, N);
}
```

```cpp
#include <hip/hip_runtime.h>
#include <stdint.h>
#include <algorithm>

#define NSLICE 2
#define ST_AUX 16
#define DETERMINISTIC 1

typedef unsigned short u16;
typedef unsigned int u32;
typedef short s4 __attribute__((ext_vector_type(4)));
typedef short s8 __attribute__((ext_vector_type(8)));
typedef __bf16 bf2 __attribute__((ext_vector_type(2)));
typedef __bf16 bf8 __attribute__((ext_vector_type(8)));
typedef float f2 __attribute__((ext_vector_type(2)));
typedef float f4 __attribute__((ext_vector_type(4)));
typedef float f16v __attribute__((ext_vector_type(16)));
typedef u32 u2 __attribute__((ext_vector_type(2)));
typedef u32 u4 __attribute__((ext_vector_type(4)));
typedef __attribute__((address_space(3))) s4* lds_s4p;
typedef __attribute__((address_space(3))) void* lds_vp;

constexpr int S = NSLICE;
constexpr int G = 64 / S;
constexpr int NB = 16 * S;
constexpr int CAPC = (4 * S > 8) ? 4 * S : 8;
constexpr bool USE_RV = (S >= 4);
constexpr int TM = 64;
constexpr int K1T = 512;
constexpr int NFILT_MIN = 60;
constexpr int EPT = 16;
constexpr int CHMAX = K1T * EPT;
constexpr int LDS_BYTES = 163840;
constexpr int OFF_W2 = 0, OFF_W1 = 98304, OFF_S = 131072, OFF_H = 147456, OFF_V = 98304;
constexpr int RPITCH = 1296;
constexpr int POS_BYTES = 81920;
constexpr float CUTOFF = 5.0f;
constexpr int K2T = 256;
constexpr int TCAP = (S >= 8) ? 1024 : (S == 4 ? 448 : (S == 2 ? 320 : 192));
constexpr int CAPN = (S >= 4) ? 32 : 48;
constexpr u32 OOB = 0x80000000u;
constexpr u32 INV = 0xffffffffu;
constexpr u32 MORE = 0xfffffffeu;

__device__ __forceinline__ float lo2f(u32 x) { return __uint_as_float(x << 16); }
__device__ __forceinline__ float hi2f(u32 x) { return __uint_as_float(x & 0xffff0000u); }
__device__ __forceinline__ u32 pack2(float a, float b) {
    bf2 r = {(__bf16)a, (__bf16)b};
    return __builtin_bit_cast(u32, r);
}
__device__ __forceinline__ void dma16(const void* g, void* l) {
    __builtin_amdgcn_global_load_lds((const u32*)g, (u32*)l, 16, 0, 0);
}
__device__ __forceinline__ s4 tr_read(const char* p) {
    return __builtin_amdgcn_ds_read_tr16_b64_v4i16((lds_s4p)(lds_vp)(p));
}
__device__ __forceinline__ bf8 cat8(s4 a, s4 b) {
    s8 r = __builtin_shufflevector(a, b, 0, 1, 2, 3, 4, 5, 6, 7);
    return __builtin_bit_cast(bf8, r);
}
__device__ __forceinline__ u32 mbcnt64(unsigned long long m) {
    return __builtin_amdgcn_mbcnt_hi((u32)(m >> 32), __builtin_amdgcn_mbcnt_lo((u32)m, 0));
}
__device__ __forceinline__ int wave_incl_scan(int v) {
    v += __builtin_amdgcn_update_dpp(0, v, 0x111, 0xf, 0xf, false);
    v += __builtin_amdgcn_update_dpp(0, v, 0x112, 0xf, 0xf, false);
    v += __builtin_amdgcn_update_dpp(0, v, 0x114, 0xf, 0xf, false);
    v += __builtin_amdgcn_update_dpp(0, v, 0x118, 0xf, 0xf, false);
    v += __builtin_amdgcn_update_dpp(0, v, 0x142, 0xa, 0xf, false);
    v += __builtin_amdgcn_update_dpp(0, v, 0x143, 0xc, 0xf, false);
    return v;
}
__device__ __forceinline__ void lds_barrier() {
    asm volatile("s_waitcnt lgkmcnt(0)" ::: "memory");
    __builtin_amdgcn_s_barrier();
    asm volatile("" ::: "memory");
}

#define STAMP(idx)
__device__ __forceinline__ void gemm_role(char* lds, int tile, const u16* __restrict__ sv, const u16* __restrict__ w1,
                                          const u16* __restrict__ w2, char* __restrict__ tmain, char* __restrict__ tz,
                                          char* __restrict__ rv, int N) {
    const int tid = threadIdx.x, lane = tid & 63, w = __builtin_amdgcn_readfirstlane(tid >> 6);
    const int n0 = tile * TM;
    const int stamp_base = 0;
    STAMP(0);
    const int l4 = lane >> 4, l15 = lane & 15;
#pragma unroll
    for (int i = 0; i < 2; ++i) {
        const int row = (2 * w + i) * 4 + l4, c = l15 ^ (row & 15);
        const int node = min(n0 + row, N - 1);
        dma16((const char*)sv + (size_t)node * 1024 + c * 16, lds + OFF_S + (2 * w + i) * 1024);
    }
    const u32 csw = (((l15 >> 2) ^ l4) << 6) | ((l15 & 3) << 4);
    {
        const char* g1 = (const char*)w1 + l4 * 256 + csw;
#pragma unroll
        for (int i = 0; i < 4; ++i) dma16(g1 + (4 * w + i) * 1024, lds + OFF_W1 + (4 * w + i) * 1024);
        const char* g2 = (const char*)w2 + l4 * 768 + csw;
#pragma unroll
        for (int t = 0; t < 3; ++t)
#pragma unroll
            for (int i = 0; i < 4; ++i) dma16(g2 + (4 * w + i) * 3072 + t * 256, lds + OFF_W2 + t * 32768 + (4 * w + i) * 1024);
    }
    u32 warm = 0;
    if (!USE_RV && w < 6) {
        const int ln = w * 64 + lane, nl = ln / 6, part = ln - nl * 6;
        const char* p = (const char*)sv + (size_t)min(n0 + nl, N - 1) * 1024 + 256 + part * 128;
        asm volatile("global_load_dword %0, %1, off" : "=v"(warm) : "v"(p) : "memory");
    }
    __builtin_amdgcn_sched_barrier(0);
    u32 vreg[8][4];
    if (USE_RV) {
#pragma unroll
        for (int i = 0; i < 8; ++i) {
            const int node = min(n0 + 8 * w + i, N - 1);
            const char* p = (const char*)sv + (size_t)node * 1024;
#pragma unroll
            for (int q = 0; q < 4; ++q) {
                u32 t;
                asm volatile("global_load_dword %0, %1, %2 offset:%3" : "=v"(t) : "v"(lane * 4), "s"(p), "i"(q * 256) : "memory");
                vreg[i][q] = t;
            }
        }
    }
    __builtin_amdgcn_sched_barrier(0);
    if (USE_RV) asm volatile("s_waitcnt vmcnt(44)" ::: "memory");
    else if (w < 6) asm volatile("s_waitcnt vmcnt(13)" ::: "memory");
    else asm volatile("s_waitcnt vmcnt(12)" ::: "memory");
    __builtin_amdgcn_s_barrier();
    __builtin_amdgcn_sched_barrier(0);
    STAMP(1);

    const int r = lane & 31, h = lane >> 5, g = lane >> 4, tq = (lane & 15) >> 2, tp = lane & 3;
    const int nt = w & 1;
    const int row = 32 * nt + r;
    const int xsw = (h ^ (r & 15)) << 4;
    const int trl = (8 * (g >> 1) + tq) * 256 + 32 * (g & 1) + 8 * tp;
    {
        const int ht = w >> 1;
        const char* abase = lds + OFF_W1 + trl + ((ht ^ tq) << 6);
        const char* bbase = lds + OFF_S + row * 256;
        f16v acc = {};
#pragma unroll
        for (int ks = 0; ks < 8; ++ks) {
            s4 a0 = tr_read(abase + ks * 4096), a1 = tr_read(abase + ks * 4096 + 1024);
            bf8 b = *(const bf8*)(bbase + ((ks * 32) ^ xsw));
            acc = __builtin_amdgcn_mfma_f32_32x32x16_bf16(cat8(a0, a1), b, acc, 0, 0, 0);
        }
        char* hbase = lds + OFF_H + row * 256 + 8 * h;
#pragma unroll
        for (int gg = 0; gg < 4; ++gg) {
            float y[4];
#pragma unroll
            for (int j = 0; j < 4; ++j) { float a = acc[4 * gg + j]; y[j] = a * __builtin_amdgcn_rcpf(1.0f + __expf(-a)); }
            u2 o = {pack2(y[0], y[1]), pack2(y[2], y[3])};
            *(u2*)(hbase + (((4 * ht + gg) ^ (r & 15)) << 4)) = o;
        }
    }
    STAMP(2);
    asm volatile("s_waitcnt vmcnt(0) lgkmcnt(0)" ::: "memory");
    asm volatile("" :: "v"(warm));
    __builtin_amdgcn_s_barrier();
    __builtin_amdgcn_sched_barrier(0);
    STAMP(3);
#pragma unroll
    for (int i = 0; i < 6; ++i) {
        const int rr = (6 * w + i) * 4 + l4;
        const int nl = rr / 3, q = rr - nl * 3;
        const int node = min(n0 + nl, N - 1);
        dma16((const char*)sv + (size_t)node * 1024 + (q + 1) * 256 + ((l15 ^ (nl & 15)) << 4), lds + OFF_V + (6 * w + i) * 1024);
    }
    if (USE_RV) {
        auto rr = __builtin_amdgcn_make_buffer_rsrc((void*)rv, 0, (u32)N * 1024u, 0x00020000);
        const int sl = lane / G, gl = lane % G;
        const u32 lo = (u32)sl * N * (16 * G) + gl * 4;
#pragma unroll
        for (int i = 0; i < 8; ++i) {
            const int node = n0 + 8 * w + i;
            const u32 lo_ = (node < N) ? lo : OOB;
#pragma unroll
            for (int q = 0; q < 4; ++q) __builtin_amdgcn_raw_buffer_store_b32(vreg[i][q], rr, lo_ + q * (4 * G), min(node, N - 1) * (16 * G), ST_AUX);
        }
    }
    f16v acc2[3] = {};
    const int cb = w >> 1;
    {
        const char* bbase = lds + OFF_H + row * 256;
        const char* ab = lds + OFF_W2 + trl + ((cb ^ tq) << 6);
#pragma unroll
        for (int ks = 0; ks < 8; ++ks) {
            bf8 b = *(const bf8*)(bbase + ((ks * 32) ^ xsw));
#pragma unroll
            for (int t = 0; t < 3; ++t) {
                s4 a0 = tr_read(ab + t * 32768 + ks * 4096), a1 = tr_read(ab + t * 32768 + ks * 4096 + 1024);
                acc2[t] = __builtin_amdgcn_mfma_f32_32x32x16_bf16(cat8(a0, a1), b, acc2[t], 0, 0, 0);
            }
        }
    }
    if (USE_RV) asm volatile("s_waitcnt vmcnt(32) lgkmcnt(0)" ::: "memory"); else asm volatile("s_waitcnt vmcnt(0) lgkmcnt(0)" ::: "memory");
    __builtin_amdgcn_s_barrier();
    __builtin_amdgcn_sched_barrier(0);
    STAMP(4);
    {
        char* rrow = lds + row * RPITCH;
        const char* vrow = lds + OFF_V + row * 768 + 8 * h;
#pragma unroll
        for (int gg = 0; gg < 4; ++gg) {
            const int csw2 = ((4 * cb + gg) ^ (r & 15)) << 4;
            const u2 vx = *(const u2*)(vrow + csw2), vy = *(const u2*)(vrow + 256 + csw2), vz = *(const u2*)(vrow + 512 + csw2);
            const float a0 = acc2[1][4 * gg], a1 = acc2[1][4 * gg + 1], a2 = acc2[1][4 * gg + 2], a3 = acc2[1][4 * gg + 3];
            const int c0 = 32 * cb + 8 * gg + 4 * h;
            u4 o0 = {pack2(acc2[0][4 * gg], acc2[0][4 * gg + 1]), pack2(acc2[2][4 * gg], acc2[2][4 * gg + 1]),
                     pack2(a0 * lo2f(vx[0]), a1 * hi2f(vx[0])), pack2(a0 * lo2f(vy[0]), a1 * hi2f(vy[0]))};
            u4 o1 = {pack2(acc2[0][4 * gg + 2], acc2[0][4 * gg + 3]), pack2(acc2[2][4 * gg + 2], acc2[2][4 * gg + 3]),
                     pack2(a2 * lo2f(vx[1]), a3 * hi2f(vx[1])), pack2(a2 * lo2f(vy[1]), a3 * hi2f(vy[1]))};
            u2 oz = {pack2(a0 * lo2f(vz[0]), a1 * hi2f(vz[0])), pack2(a2 * lo2f(vz[1]), a3 * hi2f(vz[1]))};
            *(u4*)(rrow + c0 * 8) = o0;
            *(u4*)(rrow + c0 * 8 + 16) = o1;
            *(u2*)(rrow + 1024 + c0 * 2) = oz;
        }
    }
    lds_barrier();
    __builtin_amdgcn_sched_barrier(0);
    STAMP(5);
    {
        auto rm = __builtin_amdgcn_make_buffer_rsrc((void*)tmain, 0, (u32)N * 1024u, 0x00020000);
        const int gq = lane / G, gl = lane % G;
#pragma unroll
        for (int i = 0; i < 8; ++i) {
            const int p = w * 8 + i;
            const int sl = p % S, nb = (p / S) * S;
            const int nn = nb + gq;
            u4 v = *(const u4*)(lds + nn * RPITCH + (sl * G + gl) * 16);
            const int node = n0 + nn;
            u32 off = (node < N) ? ((u32)(sl * N + node) * G + gl) * 16u : OOB;
            __builtin_amdgcn_raw_buffer_store_b128(v, rm, off, 0, ST_AUX);
        }
        auto rz = __builtin_amdgcn_make_buffer_rsrc((void*)tz, 0, (u32)N * 256u, 0x00020000);
#pragma unroll
        for (int i = 0; i < 2; ++i) {
            const int q = (w * 2 + i) * 64 + lane;
            constexpr int CPN = G / 4;
            const int sl = q / (64 * CPN), rem = q % (64 * CPN), nn = rem / CPN, ck = rem % CPN;
            u4 v = *(const u4*)(lds + nn * RPITCH + 1024 + (sl * G + ck * 4) * 4);
            const int node = n0 + nn;
            u32 off = (node < N) ? ((u32)(sl * N + node) * G + ck * 4) * 4u : OOB;
            __builtin_amdgcn_raw_buffer_store_b128(v, rz, off, 0, ST_AUX);
        }
    }
    STAMP(6);
}

template <bool POS_LDS>
__device__ __forceinline__ void get_pos(const char* lds, const u16* __restrict__ pos, u32 n, float& x, float& y, float& z) {
    if (POS_LDS) {
        const u32* pp = (const u32*)(lds + ((n * 6u) & ~3u));
        const u32 d0 = pp[0], d1 = pp[1];
        const u32 sh = n << 4;
        const u32 xy = __builtin_amdgcn_alignbit(d1, d0, sh);
        const u32 zz = d1 >> (sh & 31);
        x = lo2f(xy); y = hi2f(xy); z = lo2f(zz);
    } else {
        x = __uint_as_float((u32)pos[3 * n] << 16);
        y = __uint_as_float((u32)pos[3 * n + 1] << 16);
        z = __uint_as_float((u32)pos[3 * n + 2] << 16);
    }
}

template <bool POS_LDS>
__device__ __forceinline__ void filter_role(char* lds, int f, int chunk, const u16* __restrict__ pos, const int* __restrict__ src,
                                            const int* __restrict__ dst, u4* __restrict__ cells, u32* __restrict__ cnts, u4* __restrict__ ovf,
                                            u32* __restrict__ ovfp, u32* __restrict__ ovfn, int N, int E, int P) {
    const int tid = threadIdx.x, lane = tid & 63, w = __builtin_amdgcn_readfirstlane(tid >> 6);
    const int stamp_base = 0;
    STAMP(0);
    const int e0 = f * chunk, n = min(E, e0 + chunk) - e0;
    u32* eS = (u32*)(lds + POS_BYTES);
    u32* eD = eS + chunk;
    u16* queue = (u16*)(eD + chunk);
    u32* ocnt = (u32*)(lds + POS_BYTES + chunk * 10);
    u32* hist = ocnt + 4;
    {
        const int n4 = n >> 2;
        for (int q0 = w * 64; q0 < n4; q0 += K1T) {
            const int q = q0 + lane;
            if (q < n4) { dma16(src + e0 + q * 4, (char*)eS + q0 * 16); dma16(dst + e0 + q * 4, (char*)eD + q0 * 16); }
        }
        if (tid < 4) {
            const int e = n4 * 4 + tid;
            if (e < chunk) { eS[e] = e < n ? src[e0 + e] : 0; eD[e] = e < n ? dst[e0 + e] : 0; }
        }
    }
    if (POS_LDS) {
        const int bytes = N * 6, nch = bytes >> 4;
        for (int p = w; p * 64 < nch; p += 8) {
            int q = p * 64 + lane;
            if (q < nch) dma16((const char*)pos + q * 16, lds + p * 1024);
        }
        const int rem = (bytes - (nch << 4)) >> 1;
        if (tid < rem) ((u16*)lds)[nch * 8 + tid] = pos[nch * 8 + tid];
    }
    for (int i = tid; i < P; i += K1T) hist[i] = 0;
    if (tid == 0) { ocnt[0] = 0; ocnt[1] = 0; }
    asm volatile("s_waitcnt vmcnt(0) lgkmcnt(0)" ::: "memory");
    __builtin_amdgcn_s_barrier();
    asm volatile("" ::: "memory");
    STAMP(1);
    u32 actmask = 0;
#pragma unroll
    for (int it = 0; it < 4; ++it) {
        const int e = (it * K1T + tid) * 4;
        if (e < n) {
            const u4 a = *(const u4*)(eS + e), b = *(const u4*)(eD + e);
            u32 m4 = 0;
#pragma unroll
            for (int j = 0; j < 4; ++j) {
                float ax, ay, az, bx, by, bz;
                get_pos<POS_LDS>(lds, pos, a[j], ax, ay, az);
                get_pos<POS_LDS>(lds, pos, b[j], bx, by, bz);
                const float rx = ax - bx, ry = ay - by, rz = az - bz;
                const float d2 = __builtin_fmaf(rz, rz, __builtin_fmaf(ry, ry, __builtin_fmaf(rx, rx, 1e-8f)));
                m4 |= (d2 >= CUTOFF * CUTOFF && d2 <= 3.0e38f) ? 0u : (1u << j);
            }
            if (n - e < 4) m4 &= (1u << (n - e)) - 1u;
            actmask |= m4 << (it * 4);
        }
    }
    STAMP(2);
    {
        const int cntt = __popc(actmask);
        const int inc = wave_incl_scan(cntt);
        u32 qb = 0;
        if (lane == 63) qb = atomicAdd(&ocnt[1], (u32)inc);
        qb = __builtin_amdgcn_readlane(qb, 63) + (inc - cntt);
        u32 m = actmask;
        while (m) {
            const int bit = __builtin_ctz(m); m &= m - 1;
            queue[qb++] = (u16)((((bit >> 2) * K1T + tid) << 2) + (bit & 3));
        }
    }
    lds_barrier();
    STAMP(3);
    const int qn = ocnt[1];
    u4* cl = cells + (size_t)f * P * CAPC;
    for (int j = tid; j < qn; j += K1T) {
        const int idx = queue[j];
        const u32 s = eS[idx], dd = eD[idx];
        const u32 p = dd / NB;
        const u32 rank = atomicAdd(&hist[p], 1u);
        float ax, ay, az, bx, by, bz;
        get_pos<POS_LDS>(lds, pos, s, ax, ay, az);
        get_pos<POS_LDS>(lds, pos, dd, bx, by, bz);
        const u4 r = {s | ((dd - p * NB) << 24), __float_as_uint(ax - bx), __float_as_uint(ay - by), __float_as_uint(az - bz)};
        if (rank < CAPC) cl[p * CAPC + rank] = r;
        else { const u32 o = atomicAdd(&ocnt[0], 1u); ovf[(size_t)f * chunk + o] = r; ovfp[(size_t)f * chunk + o] = p; }
    }
    lds_barrier();
    for (int p = tid; p < P; p += K1T) cnts[(size_t)f * P + p] = hist[p];
    if (tid == 0) ovfn[f] = ocnt[0];
    STAMP(4);
}

__global__ __launch_bounds__(K1T) void k1_kernel(const u16* __restrict__ sv, const u16* __restrict__ pos, const int* __restrict__ src,
                                                 const int* __restrict__ dst, const u16* __restrict__ w1, const u16* __restrict__ w2,
                                                 char* __restrict__ tmain, char* __restrict__ tz, char* __restrict__ rv,
                                                 u4* __restrict__ cells, u32* __restrict__ cnts, u4* __restrict__ ovf, u32* __restrict__ ovfp, u32* __restrict__ ovfn,
                                                 int N, int E, int nfilt, int chunk, int P) {
    __shared__ __attribute__((aligned(1024))) char lds[LDS_BYTES];
    const int b = blockIdx.x;
    if (b < nfilt) {
        if (N * 6 + 16 <= POS_BYTES) filter_role<true>(lds, b, chunk, pos, src, dst, cells, cnts, ovf, ovfp, ovfn, N, E, P);
        else filter_role<false>(lds, b, chunk, pos, src, dst, cells, cnts, ovf, ovfp, ovfn, N, E, P);
    } else {
        gemm_role(lds, b - nfilt, sv, w1, w2, tmain, tz, rv, N);
    }
}

template <int I> __device__ __forceinline__ u32 qb(u32 x) {
    return __builtin_amdgcn_update_dpp(0u, x, I | (I << 2) | (I << 4) | (I << 6), 0xf, 0xf, true);
}
template <int I> __device__ __forceinline__ float qbf(u32 x) { return __uint_as_float(qb<I>(x)); }
__device__ __forceinline__ float fast_sin_rev(float rev) { return __builtin_amdgcn_sinf(__builtin_amdgcn_fractf(rev)); }
__device__ __forceinline__ float fast_cos_rev(float rev) { return __builtin_amdgcn_cosf(__builtin_amdgcn_fractf(rev)); }

struct Buf { u4 d[4]; u32 z[4]; u4 rb; u32 pn; };

__global__ __launch_bounds__(K2T, 4) void k2_kernel(const u16* __restrict__ sv, const u16* __restrict__ freqs, const u16* __restrict__ wr,
                                                    const u4* __restrict__ cells, const u32* __restrict__ cnts, const u4* __restrict__ ovf,
                                                    const u32* __restrict__ ovfp, const u32* __restrict__ ovfn,
                                                    const char* __restrict__ tmain, const char* __restrict__ tz, const char* __restrict__ rv,
                                                    u16* __restrict__ out, int N, int C, int chunk, int P) {
    __shared__ u4 recsA[TCAP + 1];
    __shared__ u4 recsB[TCAP + 1];
    __shared__ u32 keys[NB * CAPN];
    __shared__ u16 Lx[NB * CAPN];
    __shared__ u16 slotL[TCAP];
    __shared__ int ncnt[NB];
    __shared__ int Tsh, tetctr, slow;
    __shared__ __attribute__((aligned(16))) u32 resL[NB * 4 * G];
    const int tid = threadIdx.x, lane = tid & 63, w = __builtin_amdgcn_readfirstlane(tid >> 6);
    const int stamp_base = 512;
    STAMP(0);
    constexpr int NP = 8 / S;
    const int x = blockIdx.x & 7, j = blockIdx.x >> 3;
    const int slice = x % S, part = x / S;
    const int p = j * NP + part;
    if (p >= P) return;
    const int n0 = p * NB;
    const int grp = lane / G, gl = lane % G;
    const int ch = slice * (128 / S) + 2 * gl;
    auto rm = __builtin_amdgcn_make_buffer_rsrc((void*)(tmain + (size_t)slice * N * (G * 16)), 0, (u32)N * (G * 16), 0x00020000);
    auto rz = __builtin_amdgcn_make_buffer_rsrc((void*)(tz + (size_t)slice * N * (G * 4)), 0, (u32)N * (G * 4), 0x00020000);

    constexpr int RPT = (64 * CAPC + K2T - 1) / K2T;
    const int nslots = C * CAPC;
    const u32 cstride = (u32)P * CAPC * 16;
    auto rc = __builtin_amdgcn_make_buffer_rsrc((void*)(cells + (size_t)p * CAPC), 0, (u32)C * cstride - (u32)p * CAPC * 16, 0x00020000);
    auto rn = __builtin_amdgcn_make_buffer_rsrc((void*)(cnts + p), 0, ((u32)C * P - p) * 4, 0x00020000);
    u4 raw[RPT]; u32 rcnt[RPT];
#pragma unroll
    for (int k = 0; k < RPT; ++k) {
        const u32 q = tid + k * K2T, c = q / CAPC;
        raw[k] = __builtin_amdgcn_raw_buffer_load_b128(rc, c * cstride + (q % CAPC) * 16, 0, 0);
        rcnt[k] = __builtin_amdgcn_raw_buffer_load_b32(rn, c * (u32)P * 4, 0, 0);
    }
    __builtin_amdgcn_sched_barrier(0);
    if (tid < NB) ncnt[tid] = 0;
    if (tid == 64) { recsA[TCAP] = u4{0, 0, 0, 0}; recsB[TCAP] = u4{OOB, 0, 0, 0}; Tsh = 0; tetctr = 4; slow = (nslots > RPT * K2T) ? 1 : 0; }
    {
        constexpr int CPR = G / 4;
#pragma unroll
        for (int i = 0; i < 4; ++i) {
            const int q = (w * 4 + i) * 64 + lane;
            const int nl = q / (4 * CPR), rem = q % (4 * CPR);
            const int node = min(n0 + nl, N - 1);
            const char* src_ = USE_RV ? rv + ((size_t)slice * N + node) * (16 * G) + rem * 16
                                      : (const char*)sv + (size_t)node * 1024 + (rem / CPR) * 256 + slice * (G * 4) + (rem % CPR) * 16;
            dma16(src_, (char*)resL + (w * 4 + i) * 1024);
        }
    }
    u32 wk[3][4];
#pragma unroll
    for (int t = 0; t < 3; ++t)
#pragma unroll
        for (int k = 0; k < 4; ++k) wk[t][k] = *(const u32*)(wr + k * 384 + t * 128 + ch);
    float fr[4];
#pragma unroll
    for (int k = 0; k < 4; ++k) fr[k] = __uint_as_float((u32)freqs[k] << 16) * 0.15915494309189535f;
    lds_barrier();
    STAMP(1);
    auto push_raw = [&](const u4& r, bool valid) {
        const unsigned long long m = __ballot(valid);
        u32 ub = 0;
        if (lane == 0) ub = atomicAdd(&Tsh, (int)__popcll(m));
        const u32 u = __builtin_amdgcn_readfirstlane(ub) + mbcnt64(m);
        if (valid && u < TCAP) recsB[u] = r;
    };
#pragma unroll
    for (int k = 0; k < RPT; ++k) {
        const u32 q = tid + k * K2T;
        if (rcnt[k] > CAPC) slow = 2;
        push_raw(raw[k], (q % CAPC) < rcnt[k]);
    }
    lds_barrier();
    if (slow) {
        for (int q0 = RPT * K2T; q0 < nslots; q0 += K2T) {
            const int q = min(q0 + tid, nslots - 1);
            const u4 r = cells[((size_t)(q / CAPC) * P + p) * CAPC + (q % CAPC)];
            const u32 c = cnts[(size_t)(q / CAPC) * P + p];
            push_raw(r, q0 + tid < nslots && (u32)(q % CAPC) < c);
        }
        for (int c0 = 0; c0 < C; c0 += 64) {
            const bool ov = (c0 + lane < C) && cnts[(size_t)(c0 + lane) * P + p] > CAPC;
            const int nov = ov ? (int)ovfn[c0 + lane] : 0;
            unsigned long long cm = __ballot(ov);
            while (cm) {
                const int ci = __builtin_ctzll(cm); cm &= cm - 1;
                const int c = c0 + ci, no = __builtin_amdgcn_readlane(nov, ci);
                for (int i0 = 0; i0 < no; i0 += K2T) {
                    const int i = i0 + tid;
                    const bool mt = (i < no) && (ovfp[(size_t)c * chunk + i] == (u32)p);
                    u4 r = {INV, 0, 0, 0};
                    if (mt) r = ovf[(size_t)c * chunk + i];
                    push_raw(r, mt);
                }
            }
        }
        lds_barrier();
        if (tid == 0) slow = 0;
        lds_barrier();
    }
    const int T = Tsh;
    for (int u = tid; u < min(T, TCAP); u += K2T) {
        const u4 r = recsB[u];
        const int dl = r[0] >> 24;
        const int sl = atomicAdd(&ncnt[dl], 1);
        const u32 key = (r[0] & 0xffffffu) * (G * 16);
        if (sl < CAPN) keys[dl * CAPN + sl] = key; else slow = 1;
        slotL[u] = (u16)((dl << 8) | (sl & 255));
        const float rx = __uint_as_float(r[1]), ry = __uint_as_float(r[2]), rz_ = __uint_as_float(r[3]);
        const float d = __builtin_amdgcn_sqrtf(__builtin_fmaf(rz_, rz_, __builtin_fmaf(ry, ry, __builtin_fmaf(rx, rx, 1e-8f))));
        const float inv = __builtin_amdgcn_rcpf(d);
        const float fc = (d < CUTOFF) ? 0.5f * fast_cos_rev(d * (0.5f / CUTOFF)) + 0.5f : 0.0f * d;
        const float sc = inv * fc;
        float gk[4];
#pragma unroll
        for (int q = 0; q < 4; ++q) gk[q] = fast_sin_rev(d * fr[q]) * sc;
        const u32 h01 = pack2(gk[0], gk[1]), h23 = pack2(gk[2], gk[3]);
        const u32 l01 = pack2(gk[0] - lo2f(h01), gk[1] - hi2f(h01)), l23 = pack2(gk[2] - lo2f(h23), gk[3] - hi2f(h23));
        recsA[u] = u4{h01, h23, l01, l23};
        recsB[u] = u4{key, __float_as_uint(rx * inv), __float_as_uint(ry * inv), __float_as_uint(rz_ * inv)};
    }
    s4 wb[3][2];
#pragma unroll
    for (int t = 0; t < 3; ++t) {
        u2 b0 = {__builtin_amdgcn_perm(wk[t][1], wk[t][0], 0x05040100), __builtin_amdgcn_perm(wk[t][3], wk[t][2], 0x05040100)};
        u2 b1 = {__builtin_amdgcn_perm(wk[t][1], wk[t][0], 0x07060302), __builtin_amdgcn_perm(wk[t][3], wk[t][2], 0x07060302)};
        wb[t][0] = __builtin_bit_cast(s4, b0);
        wb[t][1] = __builtin_bit_cast(s4, b1);
    }
    asm volatile("s_waitcnt vmcnt(0)" ::: "memory");
    lds_barrier();
    STAMP(2);
    if (T > TCAP || slow) {
        float frs[4];
#pragma unroll
        for (int k = 0; k < 4; ++k) frs[k] = __uint_as_float((u32)freqs[k] << 16);
        for (int tet = w; tet < NB / S; tet += 4) {
            const int nl = tet * S + grp, node = n0 + nl;
            float acc[4][2] = {};
            for (int c = 0; c < 2 * C; ++c) {
                const int cc = c < C ? c : c - C;
                const u32 cn = cnts[(size_t)cc * P + p];
                const int l = c < C ? (int)min(cn, (u32)CAPC) : (cn > CAPC ? (int)ovfn[cc] : 0);
                for (int base = 0; base < l; base += 64) {
                    u4 rr = {INV, 0, 0, 0};
                    bool ok = base + lane < l;
                    if (ok) {
                        if (c < C) rr = cells[((size_t)cc * P + p) * CAPC + base + lane];
                        else if (ovfp[(size_t)cc * chunk + base + lane] == (u32)p) rr = ovf[(size_t)cc * chunk + base + lane];
                        else ok = false;
                    }
                    const int dl = rr[0] >> 24;
                    unsigned long long m = __ballot(ok && (dl / S == tet));
                    while (m) {
                        const int i = __builtin_ctzll(m); m &= m - 1;
                        const u32 q0 = __builtin_amdgcn_readlane(rr[0], i);
                        const float rx = __uint_as_float(__builtin_amdgcn_readlane(rr[1], i)), ry = __uint_as_float(__builtin_amdgcn_readlane(rr[2], i)),
                                    rz_ = __uint_as_float(__builtin_amdgcn_readlane(rr[3], i));
                        const float d = sqrtf(rx * rx + ry * ry + rz_ * rz_ + 1e-8f), inv = 1.0f / d;
                        const float fc = (d < CUTOFF) ? 0.5f * (cosf(3.14159265358979323846f * d / CUTOFF) + 1.0f) : 0.0f * d;
                        float W[3][2] = {};
#pragma unroll 1
                        for (int k = 0; k < 4; ++k) {
                            const float fk = k == 0 ? frs[0] : (k == 1 ? frs[1] : (k == 2 ? frs[2] : frs[3]));
                            const float gk = sinf(d * fk) * inv * fc;
#pragma unroll
                            for (int t = 0; t < 3; ++t) {
                                const u32 wv = k == 0 ? wk[t][0] : (k == 1 ? wk[t][1] : (k == 2 ? wk[t][2] : wk[t][3]));
                                W[t][0] += gk * lo2f(wv); W[t][1] += gk * hi2f(wv);
                            }
                        }
                        if ((int)(q0 >> 24) == nl) {
                            const u32 off = ((q0 & 0xffffffu) * G + gl) * 4u;
                            const u4 dd = __builtin_amdgcn_raw_buffer_load_b128(rm, off * 4u, 0, 0);
                            const u32 zz = __builtin_amdgcn_raw_buffer_load_b32(rz, off, 0, 0);
                            acc[0][0] += W[0][0] * lo2f(dd[0]); acc[0][1] += W[0][1] * hi2f(dd[0]);
                            const float t0 = W[2][0] * lo2f(dd[1]), t1 = W[2][1] * hi2f(dd[1]);
                            acc[1][0] += W[1][0] * lo2f(dd[2]) + t0 * rx * inv; acc[1][1] += W[1][1] * hi2f(dd[2]) + t1 * rx * inv;
                            acc[2][0] += W[1][0] * lo2f(dd[3]) + t0 * ry * inv; acc[2][1] += W[1][1] * hi2f(dd[3]) + t1 * ry * inv;
                            acc[3][0] += W[1][0] * lo2f(zz) + t0 * rz_ * inv;   acc[3][1] += W[1][1] * hi2f(zz) + t1 * rz_ * inv;
                        }
                    }
                }
            }
            if (node < N) {
#pragma unroll
                for (int q = 0; q < 4; ++q) {
                    const u32 r = *(const u32*)((const char*)sv + (size_t)node * 1024 + q * 256 + ch * 2);
                    *(u32*)((char*)out + (size_t)node * 1024 + q * 256 + ch * 2) = pack2(lo2f(r) + acc[q][0], hi2f(r) + acc[q][1]);
                }
            }
        }
        return;
    }
    for (int u = tid; u < T; u += K2T) {
        const int ds = slotL[u], dl = ds >> 8, sl = ds & 255;
        const int c = ncnt[dl];
        const u32 key = recsB[u][0];
        int rank = 0;
        for (int i = 0; i < c; ++i) { u32 ki = keys[dl * CAPN + i]; rank += (ki < key || (ki == key && i < sl)) ? 1 : 0; }
        Lx[dl * CAPN + rank] = (u16)u;
    }
    lds_barrier();
    STAMP(3);
    float as0 = 0, as1 = 0, ax0 = 0, ax1 = 0, ay0 = 0, ay1 = 0, az0 = 0, az1 = 0;
    const u32 gl16 = gl * 16;
    int i_tet = -1, i_b = 0, i_mm = 0, i_st = 0, i_c = 0, i_nl = 0;
    auto advance = [&]() -> bool {
        i_b += 4;
        if (i_b < i_mm) return true;
        if (i_tet < 0) i_tet = w;
        else {
            int t = 0;
            if (lane == 0) t = atomicAdd(&tetctr, 1);
            i_tet = __builtin_amdgcn_readfirstlane(t);
        }
        if (i_tet >= NB / S) return false;
        const int nl = i_tet * S + grp;
        i_nl = nl; i_st = nl * CAPN; i_c = ncnt[nl];
        int mm = 1;
#pragma unroll
        for (int g2 = 0; g2 < S; ++g2) mm = max(mm, __builtin_amdgcn_readlane(i_c, g2 * G));
        i_mm = mm;
        i_b = 0;
        return true;
    };
#define ISSUE1(B, I) { \
        const u32 off_ = qb<I>(B.rb[0]) + gl16; \
        B.d[I] = __builtin_amdgcn_raw_buffer_load_b128(rm, off_, 0, 0); \
        B.z[I] = __builtin_amdgcn_raw_buffer_load_b32(rz, off_ >> 2, 0, 0); }
#define ISSUE(B, OK) { \
        const int k_ = i_b + (lane & 3); \
        int idx_ = Lx[min(i_st + k_, NB * CAPN - 1)]; \
        idx_ = ((OK) && k_ < i_c) ? idx_ : TCAP; \
        B.pn = (u32)idx_ | ((u32)i_nl << 16); \
        B.rb = recsB[idx_]; \
        ISSUE1(B, 0) ISSUE1(B, 1) ISSUE1(B, 2) ISSUE1(B, 3) \
        C_##B##_n = (OK) ? min(4, i_mm - i_b) : 0; }
#define OPQ(X) asm volatile("" : "+v"(X))
#define STEP(B, I) { \
        const float rx = qbf<I>(B.rb[1]), ry = qbf<I>(B.rb[2]), rz_ = qbf<I>(B.rb[3]); \
        const float t0 = Wr0[I] * lo2f(B.d[I][1]), t1 = Wr1[I] * hi2f(B.d[I][1]); \
        as0 = __builtin_fmaf(Ws0[I], lo2f(B.d[I][0]), as0); OPQ(as0); as1 = __builtin_fmaf(Ws1[I], hi2f(B.d[I][0]), as1); OPQ(as1); \
        ax0 = __builtin_fmaf(Wv0[I], lo2f(B.d[I][2]), ax0); OPQ(ax0); ax1 = __builtin_fmaf(Wv1[I], hi2f(B.d[I][2]), ax1); OPQ(ax1); \
        ay0 = __builtin_fmaf(Wv0[I], lo2f(B.d[I][3]), ay0); OPQ(ay0); ay1 = __builtin_fmaf(Wv1[I], hi2f(B.d[I][3]), ay1); OPQ(ay1); \
        az0 = __builtin_fmaf(Wv0[I], lo2f(B.z[I]), az0); OPQ(az0);    az1 = __builtin_fmaf(Wv1[I], hi2f(B.z[I]), az1); OPQ(az1); \
        ax0 = __builtin_fmaf(t0, rx, ax0); OPQ(ax0); ax1 = __builtin_fmaf(t1, rx, ax1); OPQ(ax1); \
        ay0 = __builtin_fmaf(t0, ry, ay0); OPQ(ay0); ay1 = __builtin_fmaf(t1, ry, ay1); OPQ(ay1); \
        az0 = __builtin_fmaf(t0, rz_, az0); OPQ(az0); az1 = __builtin_fmaf(t1, rz_, az1); OPQ(az1); }
#define WC(T, C2) __builtin_amdgcn_mfma_f32_4x4x4bf16_1k(ghi, wb[T][C2], __builtin_amdgcn_mfma_f32_4x4x4bf16_1k(glo, wb[T][C2], zf_, 0, 0, 0), 0, 0, 0)
#define COMPUTE(B) { \
        const f4 zf_ = {0, 0, 0, 0}; \
        const u4 ga_ = recsA[B.pn & 0xffffu]; \
        const u2 ghi_ = {ga_[0], ga_[1]}, glo_ = {ga_[2], ga_[3]}; \
        const s4 ghi = __builtin_bit_cast(s4, ghi_), glo = __builtin_bit_cast(s4, glo_); \
        const f4 Ws0 = WC(0, 0), Ws1 = WC(0, 1), Wv0 = WC(1, 0), Wv1 = WC(1, 1), Wr0 = WC(2, 0), Wr1 = WC(2, 1); \
        asm volatile("" :: "v"(B.d[0]), "v"(B.d[1]), "v"(B.d[2]), "v"(B.d[3]), "v"(B.z[0]), "v"(B.z[1]), "v"(B.z[2]), "v"(B.z[3])); \
        STEP(B, 0) \
        if (C_##B##_n > 1) STEP(B, 1) \
        if (C_##B##_n > 2) STEP(B, 2) \
        if (C_##B##_n > 3) STEP(B, 3) \
        if (C_##B##_fin) { \
            const int nl_ = B.pn >> 16, node_ = n0 + nl_; \
            if (node_ < N) { \
                const u32* rp = resL + nl_ * (4 * G) + gl; \
                const u32 r0 = rp[0], r1 = rp[G], r2 = rp[2 * G], r3 = rp[3 * G]; \
                char* o = (char*)out + (size_t)node_ * 1024 + ch * 2; \
                *(u32*)(o) = pack2(lo2f(r0) + as0, hi2f(r0) + as1); \
                *(u32*)(o + 256) = pack2(lo2f(r1) + ax0, hi2f(r1) + ax1); \
                *(u32*)(o + 512) = pack2(lo2f(r2) + ay0, hi2f(r2) + ay1); \
                *(u32*)(o + 768) = pack2(lo2f(r3) + az0, hi2f(r3) + az1); \
            } \
            as0 = as1 = ax0 = ax1 = ay0 = ay1 = az0 = az1 = 0.f; \
        } }
    {
        Buf A, B;
        int C_A_n, C_B_n; bool C_A_fin, C_B_fin;
        (void)advance();
        ISSUE(A, true) C_A_fin = (i_b + 4 >= i_mm);
        bool m2;
        do {
            const bool m1 = advance();
            ISSUE(B, m1) C_B_fin = m1 && (i_b + 4 >= i_mm);
            COMPUTE(A)
            m2 = m1 && advance();
            ISSUE(A, m2) C_A_fin = m2 && (i_b + 4 >= i_mm);
            COMPUTE(B)
        } while (m2);
    }
    STAMP(4);
}

extern "C" void kernel_launch(void* const* d_in, const int* in_sizes, int n_in, void* d_out, int out_size, void* d_ws, size_t ws_size,
                              hipStream_t stream) {
    const u16* sv = (const u16*)d_in[0];
    const u16* pos = (const u16*)d_in[1];
    const int* src = (const int*)d_in[2];
    const int* dst = (const int*)d_in[3];
    const u16* freqs = (const u16*)d_in[4];
    const u16* w1 = (const u16*)d_in[5];
    const u16* w2 = (const u16*)d_in[6];
    const u16* wr = (const u16*)d_in[7];
    const int N = in_sizes[0] / 512, E = in_sizes[2];
    const int P = (N + NB - 1) / NB;
    const int chmax = std::min(CHMAX, ((LDS_BYTES - POS_BYTES - 4 * P - 64) / 10) & ~3);
    int nfilt = NFILT_MIN;
    while ((((E + nfilt - 1) / nfilt + 3) & ~3) > chmax) ++nfilt;
    const int chunk = ((E + nfilt - 1) / nfilt + 3) & ~3;
    char* ws = (char*)d_ws;
    size_t off = 0;
    auto take = [&](size_t bytes) { char* p = ws + off; off += (bytes + 255) / 256 * 256; return p; };
    char* tmain = take((size_t)N * 1024);
    char* tz = take((size_t)N * 256);
    char* rv = take((size_t)N * 1024);
    u4* cells = (u4*)take((size_t)nfilt * P * CAPC * 16);
    u32* cnts = (u32*)take((size_t)nfilt * P * 4);
    u4* ovf = (u4*)take((size_t)nfilt * chunk * 16);
    u32* ovfp = (u32*)take((size_t)nfilt * chunk * 4);
    u32* ovfn = (u32*)take((size_t)nfilt * 4);
    const int ntiles = (N + TM - 1) / TM;
    k1_kernel<<<nfilt + ntiles, K1T, 0, stream>>>(sv, pos, src, dst, w1, w2, tmain, tz, rv, cells, cnts, ovf, ovfp, ovfn, N, E, nfilt, chunk, P);
    constexpr int NP = 8 / S;
    const int nj = (P + NP - 1) / NP;
    k2_kernel<<<8 * nj, K2T, 0, stream>>>(sv, freqs, wr, cells, cnts, ovf, ovfp, ovfn, tmain, tz, rv, (u16*)d_out, N, nfilt, chunk, P);
}
```

```cpp
#include <hip/hip_runtime.h>
#include <stdint.h>
#include <algorithm>

#define NSLICE 2
#define ST_AUX 16
#define OUT_AUX 16
#define CAPC_ 12
#define DETERMINISTIC 1

typedef unsigned short u16;
typedef unsigned int u32;
typedef short s4 __attribute__((ext_vector_type(4)));
typedef short s8 __attribute__((ext_vector_type(8)));
typedef __bf16 bf2 __attribute__((ext_vector_type(2)));
typedef __bf16 bf8 __attribute__((ext_vector_type(8)));
typedef float f2 __attribute__((ext_vector_type(2)));
typedef float f4 __attribute__((ext_vector_type(4)));
typedef float f16v __attribute__((ext_vector_type(16)));
typedef u32 u2 __attribute__((ext_vector_type(2)));
typedef u32 u4 __attribute__((ext_vector_type(4)));
typedef __attribute__((address_space(3))) s4* lds_s4p;
typedef __attribute__((address_space(3))) void* lds_vp;

constexpr int S = NSLICE;
constexpr int G = 64 / S;
#define NBV 100
#define K2TV 1024
constexpr int NB = NBV;
constexpr int CAPC = (NB <= 32) ? 12 : (NB <= 64 ? 16 : 24);
#define RVMIN 4
constexpr bool USE_RV = (S >= RVMIN);
constexpr int TM = 64;
constexpr int K1T = 512;
constexpr int NFILT_MIN = 60;
constexpr int EPT = 16;
constexpr int CHMAX = K1T * EPT;
constexpr int LDS_BYTES = 163840;
constexpr int OFF_W2 = 0, OFF_W1 = 98304, OFF_S = 131072, OFF_H = 147456, OFF_V = 98304;
constexpr int RPITCH = 1296;
constexpr int POS_BYTES = 81920;
constexpr float CUTOFF = 5.0f;
constexpr int K2T = K2TV;
constexpr int K2W = K2T / 64;
constexpr int TCAP = (NB <= 32) ? 320 : (NB <= 64 ? 512 : 832);
constexpr int CAPN = (S >= 4) ? 32 : 48;
constexpr u32 OOB = 0x80000000u;
constexpr u32 INV = 0xffffffffu;
constexpr u32 MORE = 0xfffffffeu;

__device__ __forceinline__ float lo2f(u32 x) { return __uint_as_float(x << 16); }
__device__ __forceinline__ float hi2f(u32 x) { return __uint_as_float(x & 0xffff0000u); }
__device__ __forceinline__ u32 pack2(float a, float b) {
    bf2 r = {(__bf16)a, (__bf16)b};
    return __builtin_bit_cast(u32, r);
}
__device__ __forceinline__ void dma16(const void* g, void* l) {
    __builtin_amdgcn_global_load_lds((const u32*)g, (u32*)l, 16, 0, 0);
}
__device__ __forceinline__ void dma16_hidden(const void* g, void* l) {
    unsigned keep;
    const unsigned dstl = __builtin_amdgcn_readfirstlane((unsigned)(size_t)(__attribute__((address_space(3))) char*)l);
    asm volatile("s_mov_b32 %0, m0\n\ts_mov_b32 m0, %2\n\ts_nop 0\n\tglobal_load_lds_dwordx4 %1, off\n\ts_mov_b32 m0, %0"
                 : "=&s"(keep) : "v"(g), "s"(dstl) : "memory");
}
__device__ __forceinline__ s4 tr_read(const char* p) {
    return __builtin_amdgcn_ds_read_tr16_b64_v4i16((lds_s4p)(lds_vp)(p));
}
__device__ __forceinline__ bf8 cat8(s4 a, s4 b) {
    s8 r = __builtin_shufflevector(a, b, 0, 1, 2, 3, 4, 5, 6, 7);
    return __builtin_bit_cast(bf8, r);
}
__device__ __forceinline__ u32 mbcnt64(unsigned long long m) {
    return __builtin_amdgcn_mbcnt_hi((u32)(m >> 32), __builtin_amdgcn_mbcnt_lo((u32)m, 0));
}
__device__ __forceinline__ int wave_incl_scan(int v) {
    v += __builtin_amdgcn_update_dpp(0, v, 0x111, 0xf, 0xf, false);
    v += __builtin_amdgcn_update_dpp(0, v, 0x112, 0xf, 0xf, false);
    v += __builtin_amdgcn_update_dpp(0, v, 0x114, 0xf, 0xf, false);
    v += __builtin_amdgcn_update_dpp(0, v, 0x118, 0xf, 0xf, false);
    v += __builtin_amdgcn_update_dpp(0, v, 0x142, 0xa, 0xf, false);
    v += __builtin_amdgcn_update_dpp(0, v, 0x143, 0xc, 0xf, false);
    return v;
}
__device__ __forceinline__ void lds_barrier() {
    asm volatile("s_waitcnt lgkmcnt(0)" ::: "memory");
    __builtin_amdgcn_s_barrier();
    asm volatile("" ::: "memory");
}

#define STAMP(idx)
__device__ __forceinline__ void gemm_role(char* lds, int tile, const u16* __restrict__ sv, const u16* __restrict__ w1,
                                          const u16* __restrict__ w2, char* __restrict__ tmain, char* __restrict__ tz,
                                          char* __restrict__ rv, int N) {
    const int tid = threadIdx.x, lane = tid & 63, w = __builtin_amdgcn_readfirstlane(tid >> 6);
    const int n0 = tile * TM;
    const int stamp_base = 0;
    STAMP(0);
    const int l4 = lane >> 4, l15 = lane & 15;
#pragma unroll
    for (int i = 0; i < 2; ++i) {
        const int row = (2 * w + i) * 4 + l4, c = l15 ^ (row & 15);
        const int node = min(n0 + row, N - 1);
        dma16((const char*)sv + (size_t)node * 1024 + c * 16, lds + OFF_S + (2 * w + i) * 1024);
    }
    const u32 csw = (((l15 >> 2) ^ l4) << 6) | ((l15 & 3) << 4);
    {
        const char* g1 = (const char*)w1 + l4 * 256 + csw;
#pragma unroll
        for (int i = 0; i < 4; ++i) dma16(g1 + (4 * w + i) * 1024, lds + OFF_W1 + (4 * w + i) * 1024);
        const char* g2 = (const char*)w2 + l4 * 768 + csw;
#pragma unroll
        for (int t = 0; t < 3; ++t)
#pragma unroll
            for (int i = 0; i < 4; ++i) dma16(g2 + (4 * w + i) * 3072 + t * 256, lds + OFF_W2 + t * 32768 + (4 * w + i) * 1024);
    }
    u32 warm = 0;
    if (!USE_RV && w < 6) {
        const int ln = w * 64 + lane, nl = ln / 6, part = ln - nl * 6;
        const char* p = (const char*)sv + (size_t)min(n0 + nl, N - 1) * 1024 + 256 + part * 128;
        asm volatile("global_load_dword %0, %1, off" : "=v"(warm) : "v"(p) : "memory");
    }
    __builtin_amdgcn_sched_barrier(0);
    u32 vreg[8][4];
    if (USE_RV) {
#pragma unroll
        for (int i = 0; i < 8; ++i) {
            const int node = min(n0 + 8 * w + i, N - 1);
            const char* p = (const char*)sv + (size_t)node * 1024;
#pragma unroll
            for (int q = 0; q < 4; ++q) {
                u32 t;
                asm volatile("global_load_dword %0, %1, %2 offset:%3" : "=v"(t) : "v"(lane * 4), "s"(p), "i"(q * 256) : "memory");
                vreg[i][q] = t;
            }
        }
    }
    __builtin_amdgcn_sched_barrier(0);
    if (USE_RV) asm volatile("s_waitcnt vmcnt(44)" ::: "memory");
    else if (w < 6) asm volatile("s_waitcnt vmcnt(13)" ::: "memory");
    else asm volatile("s_waitcnt vmcnt(12)" ::: "memory");
    __builtin_amdgcn_s_barrier();
    __builtin_amdgcn_sched_barrier(0);
    STAMP(1);

    const int r = lane & 31, h = lane >> 5, g = lane >> 4, tq = (lane & 15) >> 2, tp = lane & 3;
    const int nt = w & 1;
    const int row = 32 * nt + r;
    const int xsw = (h ^ (r & 15)) << 4;
    const int trl = (8 * (g >> 1) + tq) * 256 + 32 * (g & 1) + 8 * tp;
    {
        const int ht = w >> 1;
        const char* abase = lds + OFF_W1 + trl + ((ht ^ tq) << 6);
        const char* bbase = lds + OFF_S + row * 256;
        f16v acc = {};
#pragma unroll
        for (int ks = 0; ks < 8; ++ks) {
            s4 a0 = tr_read(abase + ks * 4096), a1 = tr_read(abase + ks * 4096 + 1024);
            bf8 b = *(const bf8*)(bbase + ((ks * 32) ^ xsw));
            acc = __builtin_amdgcn_mfma_f32_32x32x16_bf16(cat8(a0, a1), b, acc, 0, 0, 0);
        }
        char* hbase = lds + OFF_H + row * 256 + 8 * h;
#pragma unroll
        for (int gg = 0; gg < 4; ++gg) {
            float y[4];
#pragma unroll
            for (int j = 0; j < 4; ++j) { float a = acc[4 * gg + j]; y[j] = a * __builtin_amdgcn_rcpf(1.0f + __expf(-a)); }
            u2 o = {pack2(y[0], y[1]), pack2(y[2], y[3])};
            *(u2*)(hbase + (((4 * ht + gg) ^ (r & 15)) << 4)) = o;
        }
    }
    STAMP(2);
    asm volatile("s_waitcnt vmcnt(0) lgkmcnt(0)" ::: "memory");
    asm volatile("" :: "v"(warm));
    __builtin_amdgcn_s_barrier();
    __builtin_amdgcn_sched_barrier(0);
    STAMP(3);
#pragma unroll
    for (int i = 0; i < 6; ++i) {
        const int rr = (6 * w + i) * 4 + l4;
        const int nl = rr / 3, q = rr - nl * 3;
        const int node = min(n0 + nl, N - 1);
        dma16((const char*)sv + (size_t)node * 1024 + (q + 1) * 256 + ((l15 ^ (nl & 15)) << 4), lds + OFF_V + (6 * w + i) * 1024);
    }
    if (USE_RV) {
        auto rr = __builtin_amdgcn_make_buffer_rsrc((void*)rv, 0, (u32)N * 1024u, 0x00020000);
        const int sl = lane / G, gl = lane % G;
        const u32 lo = (u32)sl * N * (16 * G) + gl * 4;
#pragma unroll
        for (int i = 0; i < 8; ++i) {
            const int node = n0 + 8 * w + i;
            const u32 lo_ = (node < N) ? lo : OOB;
#pragma unroll
            for (int q = 0; q < 4; ++q) __builtin_amdgcn_raw_buffer_store_b32(vreg[i][q], rr, lo_ + q * (4 * G), min(node, N - 1) * (16 * G), ST_AUX);
        }
    }
    f16v acc2[3] = {};
    const int cb = w >> 1;
    {
        const char* bbase = lds + OFF_H + row * 256;
        const char* ab = lds + OFF_W2 + trl + ((cb ^ tq) << 6);
#pragma unroll
        for (int ks = 0; ks < 8; ++ks) {
            bf8 b = *(const bf8*)(bbase + ((ks * 32) ^ xsw));
#pragma unroll
            for (int t = 0; t < 3; ++t) {
                s4 a0 = tr_read(ab + t * 32768 + ks * 4096), a1 = tr_read(ab + t * 32768 + ks * 4096 + 1024);
                acc2[t] = __builtin_amdgcn_mfma_f32_32x32x16_bf16(cat8(a0, a1), b, acc2[t], 0, 0, 0);
            }
        }
    }
    if (USE_RV) asm volatile("s_waitcnt vmcnt(32) lgkmcnt(0)" ::: "memory"); else asm volatile("s_waitcnt vmcnt(0) lgkmcnt(0)" ::: "memory");
    __builtin_amdgcn_s_barrier();
    __builtin_amdgcn_sched_barrier(0);
    STAMP(4);
    {
        char* rrow = lds + row * RPITCH;
        const char* vrow = lds + OFF_V + row * 768 + 8 * h;
#pragma unroll
        for (int gg = 0; gg < 4; ++gg) {
            const int csw2 = ((4 * cb + gg) ^ (r & 15)) << 4;
            const u2 vx = *(const u2*)(vrow + csw2), vy = *(const u2*)(vrow + 256 + csw2), vz = *(const u2*)(vrow + 512 + csw2);
            const float a0 = acc2[1][4 * gg], a1 = acc2[1][4 * gg + 1], a2 = acc2[1][4 * gg + 2], a3 = acc2[1][4 * gg + 3];
            const int c0 = 32 * cb + 8 * gg + 4 * h;
            u4 o0 = {pack2(acc2[0][4 * gg], acc2[0][4 * gg + 1]), pack2(acc2[2][4 * gg], acc2[2][4 * gg + 1]),
                     pack2(a0 * lo2f(vx[0]), a1 * hi2f(vx[0])), pack2(a0 * lo2f(vy[0]), a1 * hi2f(vy[0]))};
            u4 o1 = {pack2(acc2[0][4 * gg + 2], acc2[0][4 * gg + 3]), pack2(acc2[2][4 * gg + 2], acc2[2][4 * gg + 3]),
                     pack2(a2 * lo2f(vx[1]), a3 * hi2f(vx[1])), pack2(a2 * lo2f(vy[1]), a3 * hi2f(vy[1]))};
            u2 oz = {pack2(a0 * lo2f(vz[0]), a1 * hi2f(vz[0])), pack2(a2 * lo2f(vz[1]), a3 * hi2f(vz[1]))};
            *(u4*)(rrow + c0 * 8) = o0;
            *(u4*)(rrow + c0 * 8 + 16) = o1;
            *(u2*)(rrow + 1024 + c0 * 2) = oz;
        }
    }
    lds_barrier();
    __builtin_amdgcn_sched_barrier(0);
    STAMP(5);
    {
        auto rm = __builtin_amdgcn_make_buffer_rsrc((void*)tmain, 0, (u32)N * 1024u, 0x00020000);
        const int gq = lane / G, gl = lane % G;
#pragma unroll
        for (int i = 0; i < 8; ++i) {
            const int p = w * 8 + i;
            const int sl = p % S, nb = (p / S) * S;
            const int nn = nb + gq;
            u4 v = *(const u4*)(lds + nn * RPITCH + (sl * G + gl) * 16);
            const int node = n0 + nn;
            u32 off = (node < N) ? ((u32)(sl * N + node) * G + gl) * 16u : OOB;
            __builtin_amdgcn_raw_buffer_store_b128(v, rm, off, 0, ST_AUX);
        }
        auto rz = __builtin_amdgcn_make_buffer_rsrc((void*)tz, 0, (u32)N * 256u, 0x00020000);
#pragma unroll
        for (int i = 0; i < 2; ++i) {
            const int q = (w * 2 + i) * 64 + lane;
            constexpr int CPN = G / 4;
            const int sl = q / (64 * CPN), rem = q % (64 * CPN), nn = rem / CPN, ck = rem % CPN;
            u4 v = *(const u4*)(lds + nn * RPITCH + 1024 + (sl * G + ck * 4) * 4);
            const int node = n0 + nn;
            u32 off = (node < N) ? ((u32)(sl * N + node) * G + ck * 4) * 4u : OOB;
            __builtin_amdgcn_raw_buffer_store_b128(v, rz, off, 0, ST_AUX);
        }
    }
    STAMP(6);
}

template <bool POS_LDS>
__device__ __forceinline__ void get_pos(const char* lds, const u16* __restrict__ pos, u32 n, float& x, float& y, float& z) {
    if (POS_LDS) {
        const u32* pp = (const u32*)(lds + ((n * 6u) & ~3u));
        const u32 d0 = pp[0], d1 = pp[1];
        const u32 sh = n << 4;
        const u32 xy = __builtin_amdgcn_alignbit(d1, d0, sh);
        const u32 zz = d1 >> (sh & 31);
        x = lo2f(xy); y = hi2f(xy); z = lo2f(zz);
    } else {
        x = __uint_as_float((u32)pos[3 * n] << 16);
        y = __uint_as_float((u32)pos[3 * n + 1] << 16);
        z = __uint_as_float((u32)pos[3 * n + 2] << 16);
    }
}

template <bool POS_LDS>
__device__ __forceinline__ void filter_role(char* lds, int f, int chunk, const u16* __restrict__ pos, const int* __restrict__ src,
                                            const int* __restrict__ dst, u4* __restrict__ cells, u32* __restrict__ cnts, u4* __restrict__ ovf,
                                            u32* __restrict__ ovfp, u32* __restrict__ ovfn, int N, int E, int P) {
    const int tid = threadIdx.x, lane = tid & 63, w = __builtin_amdgcn_readfirstlane(tid >> 6);
    const int stamp_base = 0;
    STAMP(0);
    const int e0 = f * chunk, n = min(E, e0 + chunk) - e0;
    u32* eS = (u32*)(lds + POS_BYTES);
    u32* eD = eS + chunk;
    u16* queue = (u16*)(eD + chunk);
    u32* ocnt = (u32*)(lds + POS_BYTES + chunk * 10);
    u32* hist = ocnt + 4;
    {
        const int n4 = n >> 2;
        for (int q0 = w * 64; q0 < n4; q0 += K1T) {
            const int q = q0 + lane;
            if (q < n4) { dma16(src + e0 + q * 4, (char*)eS + q0 * 16); dma16(dst + e0 + q * 4, (char*)eD + q0 * 16); }
        }
        if (tid < 4) {
            const int e = n4 * 4 + tid;
            if (e < chunk) { eS[e] = e < n ? src[e0 + e] : 0; eD[e] = e < n ? dst[e0 + e] : 0; }
        }
    }
    if (POS_LDS) {
        const int bytes = N * 6, nch = bytes >> 4;
        for (int p = w; p * 64 < nch; p += 8) {
            int q = p * 64 + lane;
            if (q < nch) dma16((const char*)pos + q * 16, lds + p * 1024);
        }
        const int rem = (bytes - (nch << 4)) >> 1;
        if (tid < rem) ((u16*)lds)[nch * 8 + tid] = pos[nch * 8 + tid];
    }
    for (int i = tid; i < P; i += K1T) hist[i] = 0;
    if (tid == 0) { ocnt[0] = 0; ocnt[1] = 0; }
    asm volatile("s_waitcnt vmcnt(0) lgkmcnt(0)" ::: "memory");
    __builtin_amdgcn_s_barrier();
    asm volatile("" ::: "memory");
    STAMP(1);
    u32 actmask = 0;
#pragma unroll
    for (int it = 0; it < 4; ++it) {
        const int e = (it * K1T + tid) * 4;
        if (e < n) {
            const u4 a = *(const u4*)(eS + e), b = *(const u4*)(eD + e);
            u32 m4 = 0;
#pragma unroll
            for (int j = 0; j < 4; ++j) {
                float ax, ay, az, bx, by, bz;
                get_pos<POS_LDS>(lds, pos, a[j], ax, ay, az);
                get_pos<POS_LDS>(lds, pos, b[j], bx, by, bz);
                const float rx = ax - bx, ry = ay - by, rz = az - bz;
                const float d2 = __builtin_fmaf(rz, rz, __builtin_fmaf(ry, ry, __builtin_fmaf(rx, rx, 1e-8f)));
                m4 |= (d2 >= CUTOFF * CUTOFF && d2 <= 3.0e38f) ? 0u : (1u << j);
            }
            if (n - e < 4) m4 &= (1u << (n - e)) - 1u;
            actmask |= m4 << (it * 4);
        }
    }
    STAMP(2);
    {
        const int cntt = __popc(actmask);
        const int inc = wave_incl_scan(cntt);
        u32 qb = 0;
        if (lane == 63) qb = atomicAdd(&ocnt[1], (u32)inc);
        qb = __builtin_amdgcn_readlane(qb, 63) + (inc - cntt);
        u32 m = actmask;
        while (m) {
            const int bit = __builtin_ctz(m); m &= m - 1;
            queue[qb++] = (u16)((((bit >> 2) * K1T + tid) << 2) + (bit & 3));
        }
    }
    lds_barrier();
    STAMP(3);
    const int qn = ocnt[1];
    u4* cl = cells + (size_t)f * P * CAPC;
    for (int j = tid; j < qn; j += K1T) {
        const int idx = queue[j];
        const u32 s = eS[idx], dd = eD[idx];
        const u32 p = dd / NB;
        const u32 rank = atomicAdd(&hist[p], 1u);
        float ax, ay, az, bx, by, bz;
        get_pos<POS_LDS>(lds, pos, s, ax, ay, az);
        get_pos<POS_LDS>(lds, pos, dd, bx, by, bz);
        const u4 r = {s | ((dd - p * NB) << 24), __float_as_uint(ax - bx), __float_as_uint(ay - by), __float_as_uint(az - bz)};
        if (rank < CAPC) cl[p * CAPC + rank] = r;
        else { const u32 o = atomicAdd(&ocnt[0], 1u); ovf[(size_t)f * chunk + o] = r; ovfp[(size_t)f * chunk + o] = p; }
    }
    lds_barrier();
    for (int p = tid; p < P; p += K1T) cnts[(size_t)f * P + p] = hist[p];
    if (tid == 0) ovfn[f] = ocnt[0];
    STAMP(4);
}

__global__ __launch_bounds__(K1T) void k1_kernel(const u16* __restrict__ sv, const u16* __restrict__ pos, const int* __restrict__ src,
                                                 const int* __restrict__ dst, const u16* __restrict__ w1, const u16* __restrict__ w2,
                                                 char* __restrict__ tmain, char* __restrict__ tz, char* __restrict__ rv,
                                                 u4* __restrict__ cells, u32* __restrict__ cnts, u4* __restrict__ ovf, u32* __restrict__ ovfp, u32* __restrict__ ovfn,
                                                 int N, int E, int nfilt, int chunk, int P) {
    __shared__ __attribute__((aligned(1024))) char lds[LDS_BYTES];
    const int b = blockIdx.x;
    if (b < nfilt) {
        if (N * 6 + 16 <= POS_BYTES) filter_role<true>(lds, b, chunk, pos, src, dst, cells, cnts, ovf, ovfp, ovfn, N, E, P);
        else filter_role<false>(lds, b, chunk, pos, src, dst, cells, cnts, ovf, ovfp, ovfn, N, E, P);
    } else {
        gemm_role(lds, b - nfilt, sv, w1, w2, tmain, tz, rv, N);
    }
}

template <int I> __device__ __forceinline__ u32 qb(u32 x) {
    return __builtin_amdgcn_update_dpp(0u, x, I | (I << 2) | (I << 4) | (I << 6), 0xf, 0xf, true);
}
template <int I> __device__ __forceinline__ float qbf(u32 x) { return __uint_as_float(qb<I>(x)); }
__device__ __forceinline__ float fast_sin_rev(float rev) { return __builtin_amdgcn_sinf(__builtin_amdgcn_fractf(rev)); }
__device__ __forceinline__ float fast_cos_rev(float rev) { return __builtin_amdgcn_cosf(__builtin_amdgcn_fractf(rev)); }

struct Buf { u4 d[4]; u32 z[4]; u4 rb; u32 pn; };

__global__ __launch_bounds__(K2T, 4) void k2_kernel(const u16* __restrict__ sv, const u16* __restrict__ freqs, const u16* __restrict__ wr,
                                                    const u4* __restrict__ cells, const u32* __restrict__ cnts, const u4* __restrict__ ovf,
                                                    const u32* __restrict__ ovfp, const u32* __restrict__ ovfn,
                                                    const char* __restrict__ tmain, const char* __restrict__ tz, const char* __restrict__ rv,
                                                    u16* __restrict__ out, int N, int C, int chunk, int P) {
    __shared__ u4 recsA[TCAP + 1];
    __shared__ u4 recsB[TCAP + 1];
    __shared__ u32 keys[NB * CAPN];
    __shared__ u16 Lx[NB * CAPN];
    __shared__ u16 slotL[TCAP];
    __shared__ int ncnt[NB];
    __shared__ int Tsh, tetctr, slow, novl, ovl[8];
    __shared__ __attribute__((aligned(16))) u32 resL[K2W][2][256];
    const int tid = threadIdx.x, lane = tid & 63, w = __builtin_amdgcn_readfirstlane(tid >> 6);
    const int stamp_base = 512;
    STAMP(0);
    constexpr int NP = 8 / S;
    const int x = blockIdx.x & 7, j = blockIdx.x >> 3;
    const int slice = x % S, part = x / S;
    const int p = j * NP + part;
    if (p >= P) return;
    const int n0 = p * NB;
    const int grp = lane / G, gl = lane % G;
    const int ch = slice * (128 / S) + 2 * gl;
    auto rm = __builtin_amdgcn_make_buffer_rsrc((void*)(tmain + (size_t)slice * N * (G * 16)), 0, (u32)N * (G * 16), 0x00020000);
    auto rz = __builtin_amdgcn_make_buffer_rsrc((void*)(tz + (size_t)slice * N * (G * 4)), 0, (u32)N * (G * 4), 0x00020000);
    auto ro = __builtin_amdgcn_make_buffer_rsrc((void*)out, 0, (u32)N * 1024u, 0x00020000);

    constexpr int RPT = (64 * CAPC + K2T - 1) / K2T;
    const int nslots = C * CAPC;
    const u32 cstride = (u32)P * CAPC * 16;
    auto rc = __builtin_amdgcn_make_buffer_rsrc((void*)(cells + (size_t)p * CAPC), 0, (u32)C * cstride - (u32)p * CAPC * 16, 0x00020000);
    auto rn = __builtin_amdgcn_make_buffer_rsrc((void*)(cnts + p), 0, ((u32)C * P - p) * 4, 0x00020000);
    u4 raw[RPT]; u32 rcnt[RPT];
#pragma unroll
    for (int k = 0; k < RPT; ++k) {
        const u32 q = tid + k * K2T, c = q / CAPC;
        raw[k] = __builtin_amdgcn_raw_buffer_load_b128(rc, c * cstride + (q % CAPC) * 16, 0, 0);
        rcnt[k] = __builtin_amdgcn_raw_buffer_load_b32(rn, c * (u32)P * 4, 0, 0);
    }
    const u32 fq = freqs[lane & 3];
    __builtin_amdgcn_sched_barrier(0);
    if (tid < NB) ncnt[tid] = 0;
    if (tid == 64) { recsA[TCAP] = u4{0, 0, 0, 0}; recsB[TCAP] = u4{OOB, 0, 0, 0}; Tsh = 0; tetctr = K2W; slow = (nslots > RPT * K2T) ? 1 : 0; novl = 0; }
    u32 wk[3][4];
#pragma unroll
    for (int t = 0; t < 3; ++t)
#pragma unroll
        for (int k = 0; k < 4; ++k) wk[t][k] = *(const u32*)(wr + k * 384 + t * 128 + ch);
    lds_barrier();
    STAMP(1);
    auto push_raw = [&](const u4& r, bool valid) {
        const unsigned long long m = __ballot(valid);
        u32 ub = 0;
        if (lane == 0) ub = atomicAdd(&Tsh, (int)__popcll(m));
        const u32 u = __builtin_amdgcn_readfirstlane(ub) + mbcnt64(m);
        if (valid && u < TCAP) recsB[u] = r;
    };
#pragma unroll
    for (int k = 0; k < RPT; ++k) {
        const u32 q = tid + k * K2T;
        if (rcnt[k] > CAPC && (q % CAPC) == 0) { const int o = atomicAdd(&novl, 1); if (o < 8) ovl[o] = q / CAPC; else slow = 1; }
        push_raw(raw[k], (q % CAPC) < rcnt[k]);
    }
    lds_barrier();
    if (slow | novl) {
        if (slow) {
            lds_barrier();
            if (tid == 0) Tsh = TCAP + 1;
        } else {
            const int no_ = novl;
            for (int oi = 0; oi < no_; ++oi) {
                const int c = ovl[oi], no = ovfn[c];
                for (int i0 = 0; i0 < no; i0 += K2T) {
                    const int i = min(i0 + tid, no - 1);
                    const u32 pp = ovfp[(size_t)c * chunk + i];
                    const u4 r = ovf[(size_t)c * chunk + i];
                    push_raw(r, i0 + tid < no && pp == (u32)p);
                }
            }
        }
        lds_barrier();
    }
    float fr[4];
#pragma unroll
    for (int k = 0; k < 4; ++k) fr[k] = __uint_as_float(__builtin_amdgcn_readlane(fq, k) << 16) * 0.15915494309189535f;
    const int T = Tsh;
    for (int u = tid; u < min(T, TCAP); u += K2T) {
        const u4 r = recsB[u];
        const int dl = r[0] >> 24;
        const int sl = atomicAdd(&ncnt[dl], 1);
        const u32 key = (r[0] & 0xffffffu) * (G * 16);
        if (sl < CAPN) keys[dl * CAPN + sl] = key; else slow = 1;
        slotL[u] = (u16)((dl << 8) | (sl & 255));
        const float rx = __uint_as_float(r[1]), ry = __uint_as_float(r[2]), rz_ = __uint_as_float(r[3]);
        const float d = __builtin_amdgcn_sqrtf(__builtin_fmaf(rz_, rz_, __builtin_fmaf(ry, ry, __builtin_fmaf(rx, rx, 1e-8f))));
        const float inv = __builtin_amdgcn_rcpf(d);
        const float fc = (d < CUTOFF) ? 0.5f * fast_cos_rev(d * (0.5f / CUTOFF)) + 0.5f : 0.0f * d;
        const float sc = inv * fc;
        float gk[4];
#pragma unroll
        for (int q = 0; q < 4; ++q) gk[q] = fast_sin_rev(d * fr[q]) * sc;
        const u32 h01 = pack2(gk[0], gk[1]), h23 = pack2(gk[2], gk[3]);
        const u32 l01 = pack2(gk[0] - lo2f(h01), gk[1] - hi2f(h01)), l23 = pack2(gk[2] - lo2f(h23), gk[3] - hi2f(h23));
        recsA[u] = u4{h01, h23, l01, l23};
        recsB[u] = u4{key, __float_as_uint(rx * inv), __float_as_uint(ry * inv), __float_as_uint(rz_ * inv)};
    }
    s4 wb[3][2];
#pragma unroll
    for (int t = 0; t < 3; ++t) {
        u2 b0 = {__builtin_amdgcn_perm(wk[t][1], wk[t][0], 0x05040100), __builtin_amdgcn_perm(wk[t][3], wk[t][2], 0x05040100)};
        u2 b1 = {__builtin_amdgcn_perm(wk[t][1], wk[t][0], 0x07060302), __builtin_amdgcn_perm(wk[t][3], wk[t][2], 0x07060302)};
        wb[t][0] = __builtin_bit_cast(s4, b0);
        wb[t][1] = __builtin_bit_cast(s4, b1);
    }
    lds_barrier();
    STAMP(2);
    if (T > TCAP || slow) {
        float frs[4];
#pragma unroll
        for (int k = 0; k < 4; ++k) frs[k] = __uint_as_float((u32)freqs[k] << 16);
        for (int tet = w; tet < NB / S; tet += K2W) {
            const int nl = tet * S + grp, node = n0 + nl;
            float acc[4][2] = {};
            for (int c = 0; c < 2 * C; ++c) {
                const int cc = c < C ? c : c - C;
                const u32 cn = cnts[(size_t)cc * P + p];
                const int l = c < C ? (int)min(cn, (u32)CAPC) : (cn > CAPC ? (int)ovfn[cc] : 0);
                for (int base = 0; base < l; base += 64) {
                    u4 rr = {INV, 0, 0, 0};
                    bool ok = base + lane < l;
                    if (ok) {
                        if (c < C) rr = cells[((size_t)cc * P + p) * CAPC + base + lane];
                        else if (ovfp[(size_t)cc * chunk + base + lane] == (u32)p) rr = ovf[(size_t)cc * chunk + base + lane];
                        else ok = false;
                    }
                    const int dl = rr[0] >> 24;
                    unsigned long long m = __ballot(ok && (dl / S == tet));
                    while (m) {
                        const int i = __builtin_ctzll(m); m &= m - 1;
                        const u32 q0 = __builtin_amdgcn_readlane(rr[0], i);
                        const float rx = __uint_as_float(__builtin_amdgcn_readlane(rr[1], i)), ry = __uint_as_float(__builtin_amdgcn_readlane(rr[2], i)),
                                    rz_ = __uint_as_float(__builtin_amdgcn_readlane(rr[3], i));
                        const float d = sqrtf(rx * rx + ry * ry + rz_ * rz_ + 1e-8f), inv = 1.0f / d;
                        const float fc = (d < CUTOFF) ? 0.5f * (cosf(3.14159265358979323846f * d / CUTOFF) + 1.0f) : 0.0f * d;
                        float W[3][2] = {};
#pragma unroll 1
                        for (int k = 0; k < 4; ++k) {
                            const float fk = k == 0 ? frs[0] : (k == 1 ? frs[1] : (k == 2 ? frs[2] : frs[3]));
                            const float gk = sinf(d * fk) * inv * fc;
#pragma unroll
                            for (int t = 0; t < 3; ++t) {
                                const u32 wv = k == 0 ? wk[t][0] : (k == 1 ? wk[t][1] : (k == 2 ? wk[t][2] : wk[t][3]));
                                W[t][0] += gk * lo2f(wv); W[t][1] += gk * hi2f(wv);
                            }
                        }
                        if ((int)(q0 >> 24) == nl) {
                            const u32 off = ((q0 & 0xffffffu) * G + gl) * 4u;
                            const u4 dd = __builtin_amdgcn_raw_buffer_load_b128(rm, off * 4u, 0, 0);
                            const u32 zz = __builtin_amdgcn_raw_buffer_load_b32(rz, off, 0, 0);
                            acc[0][0] += W[0][0] * lo2f(dd[0]); acc[0][1] += W[0][1] * hi2f(dd[0]);
                            const float t0 = W[2][0] * lo2f(dd[1]), t1 = W[2][1] * hi2f(dd[1]);
                            acc[1][0] += W[1][0] * lo2f(dd[2]) + t0 * rx * inv; acc[1][1] += W[1][1] * hi2f(dd[2]) + t1 * rx * inv;
                            acc[2][0] += W[1][0] * lo2f(dd[3]) + t0 * ry * inv; acc[2][1] += W[1][1] * hi2f(dd[3]) + t1 * ry * inv;
                            acc[3][0] += W[1][0] * lo2f(zz) + t0 * rz_ * inv;   acc[3][1] += W[1][1] * hi2f(zz) + t1 * rz_ * inv;
                        }
                    }
                }
            }
            if (node < N) {
#pragma unroll
                for (int q = 0; q < 4; ++q) {
                    const u32 r = *(const u32*)((const char*)sv + (size_t)node * 1024 + q * 256 + ch * 2);
                    *(u32*)((char*)out + (size_t)node * 1024 + q * 256 + ch * 2) = pack2(lo2f(r) + acc[q][0], hi2f(r) + acc[q][1]);
                }
            }
        }
        return;
    }
    for (int u = tid; u < T; u += K2T) {
        const int ds = slotL[u], dl = ds >> 8, sl = ds & 255;
        const int c = ncnt[dl];
        const u32 key = recsB[u][0];
        int rank = 0;
        for (int i = 0; i < c; ++i) { u32 ki = keys[dl * CAPN + i]; rank += (ki < key || (ki == key && i < sl)) ? 1 : 0; }
        Lx[dl * CAPN + rank] = (u16)u;
    }
    lds_barrier();
    STAMP(3);
    float as0 = 0, as1 = 0, ax0 = 0, ax1 = 0, ay0 = 0, ay1 = 0, az0 = 0, az1 = 0;
    const u32 gl16 = gl * 16;
    int i_tet = -1, i_b = 0, i_mm = 0, i_st = 0, i_c = 0, i_nl = 0, i_par = 0;
    auto advance = [&]() -> bool {
        i_b += 4;
        if (i_b < i_mm) return true;
        if (i_tet < 0) i_tet = w;
        else {
            int t = 0;
            if (lane == 0) t = atomicAdd(&tetctr, 1);
            i_tet = __builtin_amdgcn_readfirstlane(t);
        }
        if (i_tet >= NB / S) return false;
        const int nl = i_tet * S + grp;
        i_nl = nl; i_st = nl * CAPN; i_c = ncnt[nl];
        int mm = 1;
#pragma unroll
        for (int g2 = 0; g2 < S; ++g2) mm = max(mm, __builtin_amdgcn_readlane(i_c, g2 * G));
        i_mm = mm;
        i_b = 0;
        i_par ^= 1;
        {
            constexpr int CPR = G / 4;
            const int rem = lane % (4 * CPR);
            const int node = min(n0 + i_tet * S + lane / (4 * CPR), N - 1);
            const char* src_ = USE_RV ? rv + ((size_t)slice * N + node) * (16 * G) + rem * 16
                                      : (const char*)sv + (size_t)node * 1024 + (rem / CPR) * 256 + slice * (G * 4) + (rem % CPR) * 16;
            dma16_hidden(src_, (char*)resL[w][i_par]);
        }
        return true;
    };
#define ISSUE1(B, I) { \
        const u32 off_ = qb<I>(B.rb[0]) + gl16; \
        B.d[I] = __builtin_amdgcn_raw_buffer_load_b128(rm, off_, 0, 0); \
        B.z[I] = __builtin_amdgcn_raw_buffer_load_b32(rz, off_ >> 2, 0, 0); }
#define ISSUE(B, OK) { \
        const int k_ = i_b + (lane & 3); \
        int idx_ = Lx[min(i_st + k_, NB * CAPN - 1)]; \
        idx_ = ((OK) && k_ < i_c) ? idx_ : TCAP; \
        B.pn = (u32)idx_ | ((u32)i_nl << 16); \
        B.rb = recsB[idx_]; \
        ISSUE1(B, 0) ISSUE1(B, 1) ISSUE1(B, 2) ISSUE1(B, 3) \
        C_##B##_n = (OK) ? min(4, i_mm - i_b) : 0; C_##B##_par = i_par; }
#define OPQ(X) asm volatile("" : "+v"(X))
#define STEP(B, I) { \
        const float rx = qbf<I>(B.rb[1]), ry = qbf<I>(B.rb[2]), rz_ = qbf<I>(B.rb[3]); \
        const float t0 = Wr0[I] * lo2f(B.d[I][1]), t1 = Wr1[I] * hi2f(B.d[I][1]); \
        as0 = __builtin_fmaf(Ws0[I], lo2f(B.d[I][0]), as0); OPQ(as0); as1 = __builtin_fmaf(Ws1[I], hi2f(B.d[I][0]), as1); OPQ(as1); \
        ax0 = __builtin_fmaf(Wv0[I], lo2f(B.d[I][2]), ax0); OPQ(ax0); ax1 = __builtin_fmaf(Wv1[I], hi2f(B.d[I][2]), ax1); OPQ(ax1); \
        ay0 = __builtin_fmaf(Wv0[I], lo2f(B.d[I][3]), ay0); OPQ(ay0); ay1 = __builtin_fmaf(Wv1[I], hi2f(B.d[I][3]), ay1); OPQ(ay1); \
        az0 = __builtin_fmaf(Wv0[I], lo2f(B.z[I]), az0); OPQ(az0);    az1 = __builtin_fmaf(Wv1[I], hi2f(B.z[I]), az1); OPQ(az1); \
        ax0 = __builtin_fmaf(t0, rx, ax0); OPQ(ax0); ax1 = __builtin_fmaf(t1, rx, ax1); OPQ(ax1); \
        ay0 = __builtin_fmaf(t0, ry, ay0); OPQ(ay0); ay1 = __builtin_fmaf(t1, ry, ay1); OPQ(ay1); \
        az0 = __builtin_fmaf(t0, rz_, az0); OPQ(az0); az1 = __builtin_fmaf(t1, rz_, az1); OPQ(az1); }
#define WC(T, C2) __builtin_amdgcn_mfma_f32_4x4x4bf16_1k(ghi, wb[T][C2], __builtin_amdgcn_mfma_f32_4x4x4bf16_1k(glo, wb[T][C2], zf_, 0, 0, 0), 0, 0, 0)
#define COMPUTE(B) { \
        const f4 zf_ = {0, 0, 0, 0}; \
        const u4 ga_ = recsA[B.pn & 0xffffu]; \
        const u2 ghi_ = {ga_[0], ga_[1]}, glo_ = {ga_[2], ga_[3]}; \
        const s4 ghi = __builtin_bit_cast(s4, ghi_), glo = __builtin_bit_cast(s4, glo_); \
        const f4 Ws0 = WC(0, 0), Ws1 = WC(0, 1), Wv0 = WC(1, 0), Wv1 = WC(1, 1), Wr0 = WC(2, 0), Wr1 = WC(2, 1); \
        asm volatile("" :: "v"(B.d[0]), "v"(B.d[1]), "v"(B.d[2]), "v"(B.d[3]), "v"(B.z[0]), "v"(B.z[1]), "v"(B.z[2]), "v"(B.z[3])); \
        STEP(B, 0) \
        if (C_##B##_n > 1) STEP(B, 1) \
        if (C_##B##_n > 2) STEP(B, 2) \
        if (C_##B##_n > 3) STEP(B, 3) \
        if (C_##B##_fin) { \
            const int nl_ = B.pn >> 16, node_ = n0 + nl_; \
            { \
                const u32* rp = resL[w][C_##B##_par] + grp * (4 * G) + gl; \
                const u32 r0 = rp[0], r1 = rp[G], r2 = rp[2 * G], r3 = rp[3 * G]; \
                const u32 o = (node_ < N) ? (u32)node_ * 1024u + ch * 2 : OOB; \
                __builtin_amdgcn_raw_buffer_store_b32(pack2(lo2f(r0) + as0, hi2f(r0) + as1), ro, o, 0, OUT_AUX); \
                __builtin_amdgcn_raw_buffer_store_b32(pack2(lo2f(r1) + ax0, hi2f(r1) + ax1), ro, o, 256, OUT_AUX); \
                __builtin_amdgcn_raw_buffer_store_b32(pack2(lo2f(r2) + ay0, hi2f(r2) + ay1), ro, o, 512, OUT_AUX); \
                __builtin_amdgcn_raw_buffer_store_b32(pack2(lo2f(r3) + az0, hi2f(r3) + az1), ro, o, 768, OUT_AUX); \
            } \
            as0 = as1 = ax0 = ax1 = ay0 = ay1 = az0 = az1 = 0.f; \
        } }
    {
        Buf A, B;
        int C_A_n, C_B_n, C_A_par, C_B_par; bool C_A_fin, C_B_fin;
        (void)advance();
        ISSUE(A, true) C_A_fin = (i_b + 4 >= i_mm);
        bool m2;
        do {
            const bool m1 = advance();
            ISSUE(B, m1) C_B_fin = m1 && (i_b + 4 >= i_mm);
            COMPUTE(A)
            m2 = m1 && advance();
            ISSUE(A, m2) C_A_fin = m2 && (i_b + 4 >= i_mm);
            COMPUTE(B)
        } while (m2);
    }
    STAMP(4);
}

extern "C" void kernel_launch(void* const* d_in, const int* in_sizes, int n_in, void* d_out, int out_size, void* d_ws, size_t ws_size,
                              hipStream_t stream) {
    const u16* sv = (const u16*)d_in[0];
    const u16* pos = (const u16*)d_in[1];
    const int* src = (const int*)d_in[2];
    const int* dst = (const int*)d_in[3];
    const u16* freqs = (const u16*)d_in[4];
    const u16* w1 = (const u16*)d_in[5];
    const u16* w2 = (const u16*)d_in[6];
    const u16* wr = (const u16*)d_in[7];
    const int N = in_sizes[0] / 512, E = in_sizes[2];
    const int P = (N + NB - 1) / NB;
    const int chmax = std::min(CHMAX, ((LDS_BYTES - POS_BYTES - 4 * P - 64) / 10) & ~3);
    int nfilt = NFILT_MIN;
    while ((((E + nfilt - 1) / nfilt + 3) & ~3) > chmax) ++nfilt;
    const int chunk = ((E + nfilt - 1) / nfilt + 3) & ~3;
    char* ws = (char*)d_ws;
    size_t off = 0;
    auto take = [&](size_t bytes) { char* p = ws + off; off += (bytes + 255) / 256 * 256; return p; };
    char* tmain = take((size_t)N * 1024);
    char* tz = take((size_t)N * 256);
    char* rv = take((size_t)N * 1024);
    u4* cells = (u4*)take((size_t)nfilt * P * CAPC * 16);
    u32* cnts = (u32*)take((size_t)nfilt * P * 4);
    u4* ovf = (u4*)take((size_t)nfilt * chunk * 16);
    u32* ovfp = (u32*)take((size_t)nfilt * chunk * 4);
    u32* ovfn = (u32*)take((size_t)nfilt * 4);
    const int ntiles = (N + TM - 1) / TM;
    k1_kernel<<<nfilt + ntiles, K1T, 0, stream>>>(sv, pos, src, dst, w1, w2, tmain, tz, rv, cells, cnts, ovf, ovfp, ovfn, N, E, nfilt, chunk, P);
    constexpr int NP = 8 / S;
    const int nj = (P + NP - 1) / NP;
    k2_kernel<<<8 * nj, K2T, 0, stream>>>(sv, freqs, wr, cells, cnts, ovf, ovfp, ovfn, tmain, tz, rv, (u16*)d_out, N, nfilt, chunk, P);
}
```

```cpp
#include <hip/hip_runtime.h>
#include <stdint.h>
#include <algorithm>

#define NSLICE 2
#define ST_AUX 16
#define OUT_AUX 16
#define CAPC_ 12
#define DETERMINISTIC 1

typedef unsigned short u16;
typedef unsigned int u32;
typedef short s4 __attribute__((ext_vector_type(4)));
typedef short s8 __attribute__((ext_vector_type(8)));
typedef __bf16 bf2 __attribute__((ext_vector_type(2)));
typedef __bf16 bf8 __attribute__((ext_vector_type(8)));
typedef float f2 __attribute__((ext_vector_type(2)));
typedef float f4 __attribute__((ext_vector_type(4)));
typedef float f16v __attribute__((ext_vector_type(16)));
typedef u32 u2 __attribute__((ext_vector_type(2)));
typedef u32 u4 __attribute__((ext_vector_type(4)));
typedef __attribute__((address_space(3))) s4* lds_s4p;
typedef __attribute__((address_space(3))) void* lds_vp;

constexpr int S = NSLICE;
constexpr int G = 64 / S;
#define NBV 98
#define K2TV 1024
constexpr int NB = NBV;
constexpr int CAPC = (NB <= 32) ? 12 : (NB <= 64 ? 16 : 24);
#define RVMIN 4
constexpr bool USE_RV = (S >= RVMIN);
constexpr int TM = 64;
constexpr int K1T = 512;
constexpr int NFILT_MIN = 60;
constexpr int EPT = 16;
constexpr int CHMAX = K1T * EPT;
constexpr int LDS_BYTES = 163840;
constexpr int OFF_W2 = 0, OFF_W1 = 98304, OFF_S = 131072, OFF_H = 147456, OFF_V = 98304;
constexpr int RPITCH = 1296;
constexpr int POS_BYTES = 81920;
constexpr float CUTOFF = 5.0f;
constexpr int K2T = K2TV;
constexpr int K2W = K2T / 64;
constexpr int TCAP = (NB <= 32) ? 320 : (NB <= 64 ? 512 : 832);
constexpr int CAPN = (S >= 4) ? 32 : 48;
constexpr u32 OOB = 0x80000000u;
constexpr u32 INV = 0xffffffffu;
constexpr u32 MORE = 0xfffffffeu;

__device__ __forceinline__ float lo2f(u32 x) { return __uint_as_float(x << 16); }
__device__ __forceinline__ float hi2f(u32 x) { return __uint_as_float(x & 0xffff0000u); }
__device__ __forceinline__ u32 pack2(float a, float b) {
    bf2 r = {(__bf16)a, (__bf16)b};
    return __builtin_bit_cast(u32, r);
}
__device__ __forceinline__ void dma16(const void* g, void* l) {
    __builtin_amdgcn_global_load_lds((const u32*)g, (u32*)l, 16, 0, 0);
}
__device__ __forceinline__ void dma16_hidden(const void* g, void* l) {
    unsigned keep;
    const unsigned dstl = __builtin_amdgcn_readfirstlane((unsigned)(size_t)(__attribute__((address_space(3))) char*)l);
    asm volatile("s_mov_b32 %0, m0\n\ts_mov_b32 m0, %2\n\ts_nop 0\n\tglobal_load_lds_dwordx4 %1, off\n\ts_mov_b32 m0, %0"
                 : "=&s"(keep) : "v"(g), "s"(dstl) : "memory");
}
__device__ __forceinline__ s4 tr_read(const char* p) {
    return __builtin_amdgcn_ds_read_tr16_b64_v4i16((lds_s4p)(lds_vp)(p));
}
__device__ __forceinline__ bf8 cat8(s4 a, s4 b) {
    s8 r = __builtin_shufflevector(a, b, 0, 1, 2, 3, 4, 5, 6, 7);
    return __builtin_bit_cast(bf8, r);
}
__device__ __forceinline__ u32 mbcnt64(unsigned long long m) {
    return __builtin_amdgcn_mbcnt_hi((u32)(m >> 32), __builtin_amdgcn_mbcnt_lo((u32)m, 0));
}
__device__ __forceinline__ int wave_incl_scan(int v) {
    v += __builtin_amdgcn_update_dpp(0, v, 0x111, 0xf, 0xf, false);
    v += __builtin_amdgcn_update_dpp(0, v, 0x112, 0xf, 0xf, false);
    v += __builtin_amdgcn_update_dpp(0, v, 0x114, 0xf, 0xf, false);
    v += __builtin_amdgcn_update_dpp(0, v, 0x118, 0xf, 0xf, false);
    v += __builtin_amdgcn_update_dpp(0, v, 0x142, 0xa, 0xf, false);
    v += __builtin_amdgcn_update_dpp(0, v, 0x143, 0xc, 0xf, false);
    return v;
}
__device__ __forceinline__ void lds_barrier() {
    asm volatile("s_waitcnt lgkmcnt(0)" ::: "memory");
    __builtin_amdgcn_s_barrier();
    asm volatile("" ::: "memory");
}

#define STAMP(idx)
__device__ __forceinline__ void gemm_role(char* lds, int tile, const u16* __restrict__ sv, const u16* __restrict__ w1,
                                          const u16* __restrict__ w2, char* __restrict__ tmain, char* __restrict__ tz,
                                          char* __restrict__ rv, int N) {
    const int tid = threadIdx.x, lane = tid & 63, w = __builtin_amdgcn_readfirstlane(tid >> 6);
    const int n0 = tile * TM;
    const int stamp_base = 0;
    STAMP(0);
    const int l4 = lane >> 4, l15 = lane & 15;
#pragma unroll
    for (int i = 0; i < 2; ++i) {
        const int row = (2 * w + i) * 4 + l4, c = l15 ^ (row & 15);
        const int node = min(n0 + row, N - 1);
        dma16((const char*)sv + (size_t)node * 1024 + c * 16, lds + OFF_S + (2 * w + i) * 1024);
    }
    const u32 csw = (((l15 >> 2) ^ l4) << 6) | ((l15 & 3) << 4);
    {
        const char* g1 = (const char*)w1 + l4 * 256 + csw;
#pragma unroll
        for (int i = 0; i < 4; ++i) dma16(g1 + (4 * w + i) * 1024, lds + OFF_W1 + (4 * w + i) * 1024);
        const char* g2 = (const char*)w2 + l4 * 768 + csw;
#pragma unroll
        for (int t = 0; t < 3; ++t)
#pragma unroll
            for (int i = 0; i < 4; ++i) dma16(g2 + (4 * w + i) * 3072 + t * 256, lds + OFF_W2 + t * 32768 + (4 * w + i) * 1024);
    }
    __builtin_amdgcn_sched_barrier(0);
    u32 vreg[8][4];
    if (USE_RV) {
#pragma unroll
        for (int i = 0; i < 8; ++i) {
            const int node = min(n0 + 8 * w + i, N - 1);
            const char* p = (const char*)sv + (size_t)node * 1024;
#pragma unroll
            for (int q = 0; q < 4; ++q) {
                u32 t;
                asm volatile("global_load_dword %0, %1, %2 offset:%3" : "=v"(t) : "v"(lane * 4), "s"(p), "i"(q * 256) : "memory");
                vreg[i][q] = t;
            }
        }
    }
    __builtin_amdgcn_sched_barrier(0);
    if (USE_RV) asm volatile("s_waitcnt vmcnt(44)" ::: "memory"); else asm volatile("s_waitcnt vmcnt(12)" ::: "memory");
    __builtin_amdgcn_s_barrier();
    __builtin_amdgcn_sched_barrier(0);
    STAMP(1);

    const int r = lane & 31, h = lane >> 5, g = lane >> 4, tq = (lane & 15) >> 2, tp = lane & 3;
    const int nt = w & 1;
    const int row = 32 * nt + r;
    const int xsw = (h ^ (r & 15)) << 4;
    const int trl = (8 * (g >> 1) + tq) * 256 + 32 * (g & 1) + 8 * tp;
    {
        const int ht = w >> 1;
        const char* abase = lds + OFF_W1 + trl + ((ht ^ tq) << 6);
        const char* bbase = lds + OFF_S + row * 256;
        f16v acc = {};
#pragma unroll
        for (int ks = 0; ks < 8; ++ks) {
            s4 a0 = tr_read(abase + ks * 4096), a1 = tr_read(abase + ks * 4096 + 1024);
            bf8 b = *(const bf8*)(bbase + ((ks * 32) ^ xsw));
            acc = __builtin_amdgcn_mfma_f32_32x32x16_bf16(cat8(a0, a1), b, acc, 0, 0, 0);
        }
        char* hbase = lds + OFF_H + row * 256 + 8 * h;
#pragma unroll
        for (int gg = 0; gg < 4; ++gg) {
            float y[4];
#pragma unroll
            for (int j = 0; j < 4; ++j) { float a = acc[4 * gg + j]; y[j] = a * __builtin_amdgcn_rcpf(1.0f + __expf(-a)); }
            u2 o = {pack2(y[0], y[1]), pack2(y[2], y[3])};
            *(u2*)(hbase + (((4 * ht + gg) ^ (r & 15)) << 4)) = o;
        }
    }
    STAMP(2);
    asm volatile("s_waitcnt vmcnt(0) lgkmcnt(0)" ::: "memory");
    __builtin_amdgcn_s_barrier();
    __builtin_amdgcn_sched_barrier(0);
    STAMP(3);
#pragma unroll
    for (int i = 0; i < 6; ++i) {
        const int rr = (6 * w + i) * 4 + l4;
        const int nl = rr / 3, q = rr - nl * 3;
        const int node = min(n0 + nl, N - 1);
        dma16((const char*)sv + (size_t)node * 1024 + (q + 1) * 256 + ((l15 ^ (nl & 15)) << 4), lds + OFF_V + (6 * w + i) * 1024);
    }
    if (USE_RV) {
        auto rr = __builtin_amdgcn_make_buffer_rsrc((void*)rv, 0, (u32)N * 1024u, 0x00020000);
        const int sl = lane / G, gl = lane % G;
        const u32 lo = (u32)sl * N * (16 * G) + gl * 4;
#pragma unroll
        for (int i = 0; i < 8; ++i) {
            const int node = n0 + 8 * w + i;
            const u32 lo_ = (node < N) ? lo : OOB;
#pragma unroll
            for (int q = 0; q < 4; ++q) __builtin_amdgcn_raw_buffer_store_b32(vreg[i][q], rr, lo_ + q * (4 * G), min(node, N - 1) * (16 * G), ST_AUX);
        }
    }
    f16v acc2[3] = {};
    const int cb = w >> 1;
    {
        const char* bbase = lds + OFF_H + row * 256;
        const char* ab = lds + OFF_W2 + trl + ((cb ^ tq) << 6);
#pragma unroll
        for (int ks = 0; ks < 8; ++ks) {
            bf8 b = *(const bf8*)(bbase + ((ks * 32) ^ xsw));
#pragma unroll
            for (int t = 0; t < 3; ++t) {
                s4 a0 = tr_read(ab + t * 32768 + ks * 4096), a1 = tr_read(ab + t * 32768 + ks * 4096 + 1024);
                acc2[t] = __builtin_amdgcn_mfma_f32_32x32x16_bf16(cat8(a0, a1), b, acc2[t], 0, 0, 0);
            }
        }
    }
    if (USE_RV) asm volatile("s_waitcnt vmcnt(32) lgkmcnt(0)" ::: "memory"); else asm volatile("s_waitcnt vmcnt(0) lgkmcnt(0)" ::: "memory");
    __builtin_amdgcn_s_barrier();
    __builtin_amdgcn_sched_barrier(0);
    STAMP(4);
    {
        char* rrow = lds + row * RPITCH;
        const char* vrow = lds + OFF_V + row * 768 + 8 * h;
#pragma unroll
        for (int gg = 0; gg < 4; ++gg) {
            const int csw2 = ((4 * cb + gg) ^ (r & 15)) << 4;
            const u2 vx = *(const u2*)(vrow + csw2), vy = *(const u2*)(vrow + 256 + csw2), vz = *(const u2*)(vrow + 512 + csw2);
            const float a0 = acc2[1][4 * gg], a1 = acc2[1][4 * gg + 1], a2 = acc2[1][4 * gg + 2], a3 = acc2[1][4 * gg + 3];
            const int c0 = 32 * cb + 8 * gg + 4 * h;
            u4 o0 = {pack2(acc2[0][4 * gg], acc2[0][4 * gg + 1]), pack2(acc2[2][4 * gg], acc2[2][4 * gg + 1]),
                     pack2(a0 * lo2f(vx[0]), a1 * hi2f(vx[0])), pack2(a0 * lo2f(vy[0]), a1 * hi2f(vy[0]))};
            u4 o1 = {pack2(acc2[0][4 * gg + 2], acc2[0][4 * gg + 3]), pack2(acc2[2][4 * gg + 2], acc2[2][4 * gg + 3]),
                     pack2(a2 * lo2f(vx[1]), a3 * hi2f(vx[1])), pack2(a2 * lo2f(vy[1]), a3 * hi2f(vy[1]))};
            u2 oz = {pack2(a0 * lo2f(vz[0]), a1 * hi2f(vz[0])), pack2(a2 * lo2f(vz[1]), a3 * hi2f(vz[1]))};
            *(u4*)(rrow + c0 * 8) = o0;
            *(u4*)(rrow + c0 * 8 + 16) = o1;
            *(u2*)(rrow + 1024 + c0 * 2) = oz;
        }
    }
    lds_barrier();
    __builtin_amdgcn_sched_barrier(0);
    STAMP(5);
    {
        auto rm = __builtin_amdgcn_make_buffer_rsrc((void*)tmain, 0, (u32)N * 1024u, 0x00020000);
        const int gq = lane / G, gl = lane % G;
#pragma unroll
        for (int i = 0; i < 8; ++i) {
            const int p = w * 8 + i;
            const int sl = p % S, nb = (p / S) * S;
            const int nn = nb + gq;
            u4 v = *(const u4*)(lds + nn * RPITCH + (sl * G + gl) * 16);
            const int node = n0 + nn;
            u32 off = (node < N) ? ((u32)(sl * N + node) * G + gl) * 16u : OOB;
            __builtin_amdgcn_raw_buffer_store_b128(v, rm, off, 0, ST_AUX);
        }
        auto rz = __builtin_amdgcn_make_buffer_rsrc((void*)tz, 0, (u32)N * 256u, 0x00020000);
#pragma unroll
        for (int i = 0; i < 2; ++i) {
            const int q = (w * 2 + i) * 64 + lane;
            constexpr int CPN = G / 4;
            const int sl = q / (64 * CPN), rem = q % (64 * CPN), nn = rem / CPN, ck = rem % CPN;
            u4 v = *(const u4*)(lds + nn * RPITCH + 1024 + (sl * G + ck * 4) * 4);
            const int node = n0 + nn;
            u32 off = (node < N) ? ((u32)(sl * N + node) * G + ck * 4) * 4u : OOB;
            __builtin_amdgcn_raw_buffer_store_b128(v, rz, off, 0, ST_AUX);
        }
    }
    STAMP(6);
}

template <bool POS_LDS>
__device__ __forceinline__ void get_pos(const char* lds, const u16* __restrict__ pos, u32 n, float& x, float& y, float& z) {
    if (POS_LDS) {
        const u32* pp = (const u32*)(lds + ((n * 6u) & ~3u));
        const u32 d0 = pp[0], d1 = pp[1];
        const u32 sh = n << 4;
        const u32 xy = __builtin_amdgcn_alignbit(d1, d0, sh);
        const u32 zz = d1 >> (sh & 31);
        x = lo2f(xy); y = hi2f(xy); z = lo2f(zz);
    } else {
        x = __uint_as_float((u32)pos[3 * n] << 16);
        y = __uint_as_float((u32)pos[3 * n + 1] << 16);
        z = __uint_as_float((u32)pos[3 * n + 2] << 16);
    }
}

template <bool POS_LDS>
__device__ __forceinline__ void filter_role(char* lds, int f, int chunk, const u16* __restrict__ pos, const int* __restrict__ src,
                                            const int* __restrict__ dst, u4* __restrict__ cells, u32* __restrict__ cnts, u4* __restrict__ ovf,
                                            u32* __restrict__ ovfp, u32* __restrict__ ovfn, int N, int E, int P) {
    const int tid = threadIdx.x, lane = tid & 63, w = __builtin_amdgcn_readfirstlane(tid >> 6);
    const int stamp_base = 0;
    STAMP(0);
    const int e0 = f * chunk, n = min(E, e0 + chunk) - e0;
    u32* eS = (u32*)(lds + POS_BYTES);
    u32* eD = eS + chunk;
    u16* queue = (u16*)(eD + chunk);
    u32* ocnt = (u32*)(lds + POS_BYTES + chunk * 10);
    u32* hist = ocnt + 4;
    {
        const int n4 = n >> 2;
        for (int q0 = w * 64; q0 < n4; q0 += K1T) {
            const int q = q0 + lane;
            if (q < n4) { dma16(src + e0 + q * 4, (char*)eS + q0 * 16); dma16(dst + e0 + q * 4, (char*)eD + q0 * 16); }
        }
        if (tid < 4) {
            const int e = n4 * 4 + tid;
            if (e < chunk) { eS[e] = e < n ? src[e0 + e] : 0; eD[e] = e < n ? dst[e0 + e] : 0; }
        }
    }
    if (POS_LDS) {
        const int bytes = N * 6, nch = bytes >> 4;
        for (int p = w; p * 64 < nch; p += 8) {
            int q = p * 64 + lane;
            if (q < nch) dma16((const char*)pos + q * 16, lds + p * 1024);
        }
        const int rem = (bytes - (nch << 4)) >> 1;
        if (tid < rem) ((u16*)lds)[nch * 8 + tid] = pos[nch * 8 + tid];
    }
    for (int i = tid; i < P; i += K1T) hist[i] = 0;
    if (tid == 0) { ocnt[0] = 0; ocnt[1] = 0; }
    asm volatile("s_waitcnt vmcnt(0) lgkmcnt(0)" ::: "memory");
    __builtin_amdgcn_s_barrier();
    asm volatile("" ::: "memory");
    STAMP(1);
    u32 actmask = 0;
#pragma unroll
    for (int it = 0; it < 4; ++it) {
        const int e = (it * K1T + tid) * 4;
        if (e < n) {
            const u4 a = *(const u4*)(eS + e), b = *(const u4*)(eD + e);
            u32 m4 = 0;
#pragma unroll
            for (int j = 0; j < 4; ++j) {
                float ax, ay, az, bx, by, bz;
                get_pos<POS_LDS>(lds, pos, a[j], ax, ay, az);
                get_pos<POS_LDS>(lds, pos, b[j], bx, by, bz);
                const float rx = ax - bx, ry = ay - by, rz = az - bz;
                const float d2 = __builtin_fmaf(rz, rz, __builtin_fmaf(ry, ry, __builtin_fmaf(rx, rx, 1e-8f)));
                m4 |= (d2 >= CUTOFF * CUTOFF && d2 <= 3.0e38f) ? 0u : (1u << j);
            }
            if (n - e < 4) m4 &= (1u << (n - e)) - 1u;
            actmask |= m4 << (it * 4);
        }
    }
    STAMP(2);
    {
        const int cntt = __popc(actmask);
        const int inc = wave_incl_scan(cntt);
        u32 qb = 0;
        if (lane == 63) qb = atomicAdd(&ocnt[1], (u32)inc);
        qb = __builtin_amdgcn_readlane(qb, 63) + (inc - cntt);
        u32 m = actmask;
        while (m) {
            const int bit = __builtin_ctz(m); m &= m - 1;
            queue[qb++] = (u16)((((bit >> 2) * K1T + tid) << 2) + (bit & 3));
        }
    }
    lds_barrier();
    STAMP(3);
    const int qn = ocnt[1];
    u4* cl = cells + (size_t)f * P * CAPC;
    for (int j = tid; j < qn; j += K1T) {
        const int idx = queue[j];
        const u32 s = eS[idx], dd = eD[idx];
        const u32 p = dd / NB;
        const u32 rank = atomicAdd(&hist[p], 1u);
        float ax, ay, az, bx, by, bz;
        get_pos<POS_LDS>(lds, pos, s, ax, ay, az);
        get_pos<POS_LDS>(lds, pos, dd, bx, by, bz);
        const u4 r = {s | ((dd - p * NB) << 24), __float_as_uint(ax - bx), __float_as_uint(ay - by), __float_as_uint(az - bz)};
        if (rank < CAPC) cl[p * CAPC + rank] = r;
        else { const u32 o = atomicAdd(&ocnt[0], 1u); ovf[(size_t)f * chunk + o] = r; ovfp[(size_t)f * chunk + o] = p; }
    }
    lds_barrier();
    for (int p = tid; p < P; p += K1T) cnts[(size_t)f * P + p] = hist[p];
    if (tid == 0) ovfn[f] = ocnt[0];
    STAMP(4);
}

__global__ __launch_bounds__(K1T) void k1_kernel(const u16* __restrict__ sv, const u16* __restrict__ pos, const int* __restrict__ src,
                                                 const int* __restrict__ dst, const u16* __restrict__ w1, const u16* __restrict__ w2,
                                                 char* __restrict__ tmain, char* __restrict__ tz, char* __restrict__ rv,
                                                 u4* __restrict__ cells, u32* __restrict__ cnts, u4* __restrict__ ovf, u32* __restrict__ ovfp, u32* __restrict__ ovfn,
                                                 int N, int E, int nfilt, int chunk, int P) {
    __shared__ __attribute__((aligned(1024))) char lds[LDS_BYTES];
    const int b = blockIdx.x;
    if (b < nfilt) {
        if (N * 6 + 16 <= POS_BYTES) filter_role<true>(lds, b, chunk, pos, src, dst, cells, cnts, ovf, ovfp, ovfn, N, E, P);
        else filter_role<false>(lds, b, chunk, pos, src, dst, cells, cnts, ovf, ovfp, ovfn, N, E, P);
    } else {
        gemm_role(lds, b - nfilt, sv, w1, w2, tmain, tz, rv, N);
    }
}

template <int I> __device__ __forceinline__ u32 qb(u32 x) {
    return __builtin_amdgcn_update_dpp(0u, x, I | (I << 2) | (I << 4) | (I << 6), 0xf, 0xf, true);
}
template <int I> __device__ __forceinline__ float qbf(u32 x) { return __uint_as_float(qb<I>(x)); }
__device__ __forceinline__ float fast_sin_rev(float rev) { return __builtin_amdgcn_sinf(__builtin_amdgcn_fractf(rev)); }
__device__ __forceinline__ float fast_cos_rev(float rev) { return __builtin_amdgcn_cosf(__builtin_amdgcn_fractf(rev)); }

struct Buf { u4 d[4]; u32 z[4]; u4 rb; u32 pn; };

__global__ __launch_bounds__(K2T, 4) void k2_kernel(const u16* __restrict__ sv, const u16* __restrict__ freqs, const u16* __restrict__ wr,
                                                    const u4* __restrict__ cells, const u32* __restrict__ cnts, const u4* __restrict__ ovf,
                                                    const u32* __restrict__ ovfp, const u32* __restrict__ ovfn,
                                                    const char* __restrict__ tmain, const char* __restrict__ tz, const char* __restrict__ rv,
                                                    u16* __restrict__ out, int N, int C, int chunk, int P) {
    __shared__ u4 recsA[TCAP + 1];
    __shared__ u4 recsB[TCAP + 1];
    __shared__ u32 keys[NB * CAPN];
    __shared__ u16 Lx[NB * CAPN];
    __shared__ u16 slotL[TCAP];
    __shared__ int ncnt[NB];
    __shared__ int Tsh, tetctr, slow, slow2, novl, ovl[8];
    __shared__ __attribute__((aligned(16))) u32 resL[K2W][2][256];
    const int tid = threadIdx.x, lane = tid & 63, w = __builtin_amdgcn_readfirstlane(tid >> 6);
    const int stamp_base = 512;
    STAMP(0);
    constexpr int NP = 8 / S;
    const int x = blockIdx.x & 7, j = blockIdx.x >> 3;
    const int slice = x % S, part = x / S;
    const int p = j * NP + part;
    if (p >= P) return;
    const int n0 = p * NB;
    const int grp = lane / G, gl = lane % G;
    const int ch = slice * (128 / S) + 2 * gl;
    auto rm = __builtin_amdgcn_make_buffer_rsrc((void*)(tmain + (size_t)slice * N * (G * 16)), 0, (u32)N * (G * 16), 0x00020000);
    auto rz = __builtin_amdgcn_make_buffer_rsrc((void*)(tz + (size_t)slice * N * (G * 4)), 0, (u32)N * (G * 4), 0x00020000);
    auto ro = __builtin_amdgcn_make_buffer_rsrc((void*)out, 0, (u32)N * 1024u, 0x00020000);

    constexpr int RPT = (64 * CAPC + K2T - 1) / K2T;
    const int nslots = C * CAPC;
    const u32 cstride = (u32)P * CAPC * 16;
    auto rc = __builtin_amdgcn_make_buffer_rsrc((void*)(cells + (size_t)p * CAPC), 0, (u32)C * cstride - (u32)p * CAPC * 16, 0x00020000);
    auto rn = __builtin_amdgcn_make_buffer_rsrc((void*)(cnts + p), 0, ((u32)C * P - p) * 4, 0x00020000);
    u4 raw[RPT]; u32 rcnt[RPT];
#pragma unroll
    for (int k = 0; k < RPT; ++k) {
        const u32 q = tid + k * K2T, c = q / CAPC;
        raw[k] = __builtin_amdgcn_raw_buffer_load_b128(rc, c * cstride + (q % CAPC) * 16, 0, 0);
        rcnt[k] = __builtin_amdgcn_raw_buffer_load_b32(rn, c * (u32)P * 4, 0, 0);
    }
    const u32 fq = freqs[lane & 3];
    __builtin_amdgcn_sched_barrier(0);
    if (tid < NB) ncnt[tid] = 0;
    if (tid == 64) { recsA[TCAP] = u4{0, 0, 0, 0}; recsB[TCAP] = u4{OOB, 0, 0, 0}; Tsh = 0; tetctr = K2W; slow2 = 0; slow = (nslots > RPT * K2T) ? 1 : 0; novl = 0; }
    u32 wk[3][4];
#pragma unroll
    for (int t = 0; t < 3; ++t)
#pragma unroll
        for (int k = 0; k < 4; ++k) wk[t][k] = *(const u32*)(wr + k * 384 + t * 128 + ch);
    lds_barrier();
    STAMP(1);
    auto push_raw = [&](const u4& r, bool valid) {
        const unsigned long long m = __ballot(valid);
        u32 ub = 0;
        if (lane == 0) ub = atomicAdd(&Tsh, (int)__popcll(m));
        const u32 u = __builtin_amdgcn_readfirstlane(ub) + mbcnt64(m);
        if (valid && u < TCAP) recsB[u] = r;
    };
#pragma unroll
    for (int k = 0; k < RPT; ++k) {
        const u32 q = tid + k * K2T;
        if (rcnt[k] > CAPC && (q % CAPC) == 0) { const int o = atomicAdd(&novl, 1); if (o < 8) ovl[o] = q / CAPC; else slow = 1; }
        push_raw(raw[k], (q % CAPC) < rcnt[k]);
    }
    lds_barrier();
    if (slow | novl) {
        if (slow) {
            lds_barrier();
            if (tid == 0) Tsh = TCAP + 1;
        } else {
            const int no_ = novl;
            for (int oi = 0; oi < no_; ++oi) {
                const int c = ovl[oi], no = ovfn[c];
                for (int i0 = 0; i0 < no; i0 += K2T) {
                    const int i = min(i0 + tid, no - 1);
                    const u32 pp = ovfp[(size_t)c * chunk + i];
                    const u4 r = ovf[(size_t)c * chunk + i];
                    push_raw(r, i0 + tid < no && pp == (u32)p);
                }
            }
        }
        lds_barrier();
    }
    float fr[4];
#pragma unroll
    for (int k = 0; k < 4; ++k) fr[k] = __uint_as_float(__builtin_amdgcn_readlane(fq, k) << 16) * 0.15915494309189535f;
    const int T = Tsh;
    for (int u = tid; u < min(T, TCAP); u += K2T) {
        const u4 r = recsB[u];
        const int dl = r[0] >> 24;
        const int sl = atomicAdd(&ncnt[dl], 1);
        const u32 key = (r[0] & 0xffffffu) * (G * 16);
        if (sl < CAPN) keys[dl * CAPN + sl] = key; else slow2 = 1;
        slotL[u] = (u16)((dl << 8) | (sl & 255));
        const float rx = __uint_as_float(r[1]), ry = __uint_as_float(r[2]), rz_ = __uint_as_float(r[3]);
        const float d = __builtin_amdgcn_sqrtf(__builtin_fmaf(rz_, rz_, __builtin_fmaf(ry, ry, __builtin_fmaf(rx, rx, 1e-8f))));
        const float inv = __builtin_amdgcn_rcpf(d);
        const float fc = (d < CUTOFF) ? 0.5f * fast_cos_rev(d * (0.5f / CUTOFF)) + 0.5f : 0.0f * d;
        const float sc = inv * fc;
        float gk[4];
#pragma unroll
        for (int q = 0; q < 4; ++q) gk[q] = fast_sin_rev(d * fr[q]) * sc;
        const u32 h01 = pack2(gk[0], gk[1]), h23 = pack2(gk[2], gk[3]);
        const u32 l01 = pack2(gk[0] - lo2f(h01), gk[1] - hi2f(h01)), l23 = pack2(gk[2] - lo2f(h23), gk[3] - hi2f(h23));
        recsA[u] = u4{h01, h23, l01, l23};
        recsB[u] = u4{key, __float_as_uint(rx * inv), __float_as_uint(ry * inv), __float_as_uint(rz_ * inv)};
    }
    s4 wb[3][2];
#pragma unroll
    for (int t = 0; t < 3; ++t) {
        u2 b0 = {__builtin_amdgcn_perm(wk[t][1], wk[t][0], 0x05040100), __builtin_amdgcn_perm(wk[t][3], wk[t][2], 0x05040100)};
        u2 b1 = {__builtin_amdgcn_perm(wk[t][1], wk[t][0], 0x07060302), __builtin_amdgcn_perm(wk[t][3], wk[t][2], 0x07060302)};
        wb[t][0] = __builtin_bit_cast(s4, b0);
        wb[t][1] = __builtin_bit_cast(s4, b1);
    }
    lds_barrier();
    STAMP(2);
    if (T > TCAP || slow2) {
        float frs[4];
#pragma unroll
        for (int k = 0; k < 4; ++k) frs[k] = __uint_as_float((u32)freqs[k] << 16);
        for (int tet = w; tet < NB / S; tet += K2W) {
            const int nl = tet * S + grp, node = n0 + nl;
            float acc[4][2] = {};
            for (int c = 0; c < 2 * C; ++c) {
                const int cc = c < C ? c : c - C;
                const u32 cn = cnts[(size_t)cc * P + p];
                const int l = c < C ? (int)min(cn, (u32)CAPC) : (cn > CAPC ? (int)ovfn[cc] : 0);
                for (int base = 0; base < l; base += 64) {
                    u4 rr = {INV, 0, 0, 0};
                    bool ok = base + lane < l;
                    if (ok) {
                        if (c < C) rr = cells[((size_t)cc * P + p) * CAPC + base + lane];
                        else if (ovfp[(size_t)cc * chunk + base + lane] == (u32)p) rr = ovf[(size_t)cc * chunk + base + lane];
                        else ok = false;
                    }
                    const int dl = rr[0] >> 24;
                    unsigned long long m = __ballot(ok && (dl / S == tet));
                    while (m) {
                        const int i = __builtin_ctzll(m); m &= m - 1;
                        const u32 q0 = __builtin_amdgcn_readlane(rr[0], i);
                        const float rx = __uint_as_float(__builtin_amdgcn_readlane(rr[1], i)), ry = __uint_as_float(__builtin_amdgcn_readlane(rr[2], i)),
                                    rz_ = __uint_as_float(__builtin_amdgcn_readlane(rr[3], i));
                        const float d = sqrtf(rx * rx + ry * ry + rz_ * rz_ + 1e-8f), inv = 1.0f / d;
                        const float fc = (d < CUTOFF) ? 0.5f * (cosf(3.14159265358979323846f * d / CUTOFF) + 1.0f) : 0.0f * d;
                        float W[3][2] = {};
#pragma unroll 1
                        for (int k = 0; k < 4; ++k) {
                            const float fk = k == 0 ? frs[0] : (k == 1 ? frs[1] : (k == 2 ? frs[2] : frs[3]));
                            const float gk = sinf(d * fk) * inv * fc;
#pragma unroll
                            for (int t = 0; t < 3; ++t) {
                                const u32 wv = k == 0 ? wk[t][0] : (k == 1 ? wk[t][1] : (k == 2 ? wk[t][2] : wk[t][3]));
                                W[t][0] += gk * lo2f(wv); W[t][1] += gk * hi2f(wv);
                            }
                        }
                        if ((int)(q0 >> 24) == nl) {
                            const u32 off = ((q0 & 0xffffffu) * G + gl) * 4u;
                            const u4 dd = __builtin_amdgcn_raw_buffer_load_b128(rm, off * 4u, 0, 0);
                            const u32 zz = __builtin_amdgcn_raw_buffer_load_b32(rz, off, 0, 0);
                            acc[0][0] += W[0][0] * lo2f(dd[0]); acc[0][1] += W[0][1] * hi2f(dd[0]);
                            const float t0 = W[2][0] * lo2f(dd[1]), t1 = W[2][1] * hi2f(dd[1]);
                            acc[1][0] += W[1][0] * lo2f(dd[2]) + t0 * rx * inv; acc[1][1] += W[1][1] * hi2f(dd[2]) + t1 * rx * inv;
                            acc[2][0] += W[1][0] * lo2f(dd[3]) + t0 * ry * inv; acc[2][1] += W[1][1] * hi2f(dd[3]) + t1 * ry * inv;
                            acc[3][0] += W[1][0] * lo2f(zz) + t0 * rz_ * inv;   acc[3][1] += W[1][1] * hi2f(zz) + t1 * rz_ * inv;
                        }
                    }
                }
            }
            if (node < N) {
#pragma unroll
                for (int q = 0; q < 4; ++q) {
                    const u32 r = *(const u32*)((const char*)sv + (size_t)node * 1024 + q * 256 + ch * 2);
                    *(u32*)((char*)out + (size_t)node * 1024 + q * 256 + ch * 2) = pack2(lo2f(r) + acc[q][0], hi2f(r) + acc[q][1]);
                }
            }
        }
        return;
    }
    for (int u = tid; u < T; u += K2T) {
        const int ds = slotL[u], dl = ds >> 8, sl = ds & 255;
        const int c = ncnt[dl];
        const u32 key = recsB[u][0];
        int rank = 0;
        for (int i = 0; i < c; ++i) { u32 ki = keys[dl * CAPN + i]; rank += (ki < key || (ki == key && i < sl)) ? 1 : 0; }
        Lx[dl * CAPN + rank] = (u16)u;
    }
    lds_barrier();
    STAMP(3);
    float as0 = 0, as1 = 0, ax0 = 0, ax1 = 0, ay0 = 0, ay1 = 0, az0 = 0, az1 = 0;
    const u32 gl16 = gl * 16;
    int i_tet = -1, i_b = 0, i_mm = 0, i_st = 0, i_c = 0, i_nl = 0, i_par = 0;
    auto advance = [&]() -> bool {
        i_b += 4;
        if (i_b < i_mm) return true;
        if (i_tet < 0) i_tet = w;
        else {
            int t = 0;
            if (lane == 0) t = atomicAdd(&tetctr, 1);
            i_tet = __builtin_amdgcn_readfirstlane(t);
        }
        if (i_tet >= NB / S) return false;
        const int nl = i_tet * S + grp;
        i_nl = nl; i_st = nl * CAPN; i_c = ncnt[nl];
        int mm = 1;
#pragma unroll
        for (int g2 = 0; g2 < S; ++g2) mm = max(mm, __builtin_amdgcn_readlane(i_c, g2 * G));
        i_mm = mm;
        i_b = 0;
        i_par ^= 1;
        {
            constexpr int CPR = G / 4;
            const int rem = lane % (4 * CPR);
            const int node = min(n0 + i_tet * S + lane / (4 * CPR), N - 1);
            const char* src_ = USE_RV ? rv + ((size_t)slice * N + node) * (16 * G) + rem * 16
                                      : (const char*)sv + (size_t)node * 1024 + (rem / CPR) * 256 + slice * (G * 4) + (rem % CPR) * 16;
            dma16_hidden(src_, (char*)resL[w][i_par]);
        }
        return true;
    };
#define ISSUE1(B, I) { \
        const u32 off_ = qb<I>(B.rb[0]) + gl16; \
        B.d[I] = __builtin_amdgcn_raw_buffer_load_b128(rm, off_, 0, 0); \
        B.z[I] = __builtin_amdgcn_raw_buffer_load_b32(rz, off_ >> 2, 0, 0); }
#define ISSUE(B, OK) { \
        const int k_ = i_b + (lane & 3); \
        int idx_ = Lx[min(i_st + k_, NB * CAPN - 1)]; \
        idx_ = ((OK) && k_ < i_c) ? idx_ : TCAP; \
        B.pn = (u32)idx_ | ((u32)i_nl << 16); \
        B.rb = recsB[idx_]; \
        ISSUE1(B, 0) ISSUE1(B, 1) ISSUE1(B, 2) ISSUE1(B, 3) \
        C_##B##_n = (OK) ? min(4, i_mm - i_b) : 0; C_##B##_par = i_par; }
#define OPQ(X) asm volatile("" : "+v"(X))
#define STEP(B, I) { \
        const float rx = qbf<I>(B.rb[1]), ry = qbf<I>(B.rb[2]), rz_ = qbf<I>(B.rb[3]); \
        const float t0 = Wr0[I] * lo2f(B.d[I][1]), t1 = Wr1[I] * hi2f(B.d[I][1]); \
        as0 = __builtin_fmaf(Ws0[I], lo2f(B.d[I][0]), as0); OPQ(as0); as1 = __builtin_fmaf(Ws1[I], hi2f(B.d[I][0]), as1); OPQ(as1); \
        ax0 = __builtin_fmaf(Wv0[I], lo2f(B.d[I][2]), ax0); OPQ(ax0); ax1 = __builtin_fmaf(Wv1[I], hi2f(B.d[I][2]), ax1); OPQ(ax1); \
        ay0 = __builtin_fmaf(Wv0[I], lo2f(B.d[I][3]), ay0); OPQ(ay0); ay1 = __builtin_fmaf(Wv1[I], hi2f(B.d[I][3]), ay1); OPQ(ay1); \
        az0 = __builtin_fmaf(Wv0[I], lo2f(B.z[I]), az0); OPQ(az0);    az1 = __builtin_fmaf(Wv1[I], hi2f(B.z[I]), az1); OPQ(az1); \
        ax0 = __builtin_fmaf(t0, rx, ax0); OPQ(ax0); ax1 = __builtin_fmaf(t1, rx, ax1); OPQ(ax1); \
        ay0 = __builtin_fmaf(t0, ry, ay0); OPQ(ay0); ay1 = __builtin_fmaf(t1, ry, ay1); OPQ(ay1); \
        az0 = __builtin_fmaf(t0, rz_, az0); OPQ(az0); az1 = __builtin_fmaf(t1, rz_, az1); OPQ(az1); }
#define WC(T, C2) __builtin_amdgcn_mfma_f32_4x4x4bf16_1k(ghi, wb[T][C2], __builtin_amdgcn_mfma_f32_4x4x4bf16_1k(glo, wb[T][C2], zf_, 0, 0, 0), 0, 0, 0)
#define COMPUTE(B) { \
        const f4 zf_ = {0, 0, 0, 0}; \
        const u4 ga_ = recsA[B.pn & 0xffffu]; \
        const u2 ghi_ = {ga_[0], ga_[1]}, glo_ = {ga_[2], ga_[3]}; \
        const s4 ghi = __builtin_bit_cast(s4, ghi_), glo = __builtin_bit_cast(s4, glo_); \
        const f4 Ws0 = WC(0, 0), Ws1 = WC(0, 1), Wv0 = WC(1, 0), Wv1 = WC(1, 1), Wr0 = WC(2, 0), Wr1 = WC(2, 1); \
        asm volatile("" :: "v"(B.d[0]), "v"(B.d[1]), "v"(B.d[2]), "v"(B.d[3]), "v"(B.z[0]), "v"(B.z[1]), "v"(B.z[2]), "v"(B.z[3])); \
        STEP(B, 0) \
        if (C_##B##_n > 1) STEP(B, 1) \
        if (C_##B##_n > 2) STEP(B, 2) \
        if (C_##B##_n > 3) STEP(B, 3) \
        if (C_##B##_fin) { \
            const int nl_ = B.pn >> 16, node_ = n0 + nl_; \
            { \
                const u32* rp = resL[w][C_##B##_par] + grp * (4 * G) + gl; \
                const u32 r0 = rp[0], r1 = rp[G], r2 = rp[2 * G], r3 = rp[3 * G]; \
                const u32 o = (node_ < N) ? (u32)node_ * 1024u + ch * 2 : OOB; \
                __builtin_amdgcn_raw_buffer_store_b32(pack2(lo2f(r0) + as0, hi2f(r0) + as1), ro, o, 0, OUT_AUX); \
                __builtin_amdgcn_raw_buffer_store_b32(pack2(lo2f(r1) + ax0, hi2f(r1) + ax1), ro, o, 256, OUT_AUX); \
                __builtin_amdgcn_raw_buffer_store_b32(pack2(lo2f(r2) + ay0, hi2f(r2) + ay1), ro, o, 512, OUT_AUX); \
                __builtin_amdgcn_raw_buffer_store_b32(pack2(lo2f(r3) + az0, hi2f(r3) + az1), ro, o, 768, OUT_AUX); \
            } \
            as0 = as1 = ax0 = ax1 = ay0 = ay1 = az0 = az1 = 0.f; \
        } }
    {
        Buf A, B;
        int C_A_n, C_B_n, C_A_par, C_B_par; bool C_A_fin, C_B_fin;
        (void)advance();
        ISSUE(A, true) C_A_fin = (i_b + 4 >= i_mm);
        bool m2;
        do {
            const bool m1 = advance();
            ISSUE(B, m1) C_B_fin = m1 && (i_b + 4 >= i_mm);
            COMPUTE(A)
            m2 = m1 && advance();
            ISSUE(A, m2) C_A_fin = m2 && (i_b + 4 >= i_mm);
            COMPUTE(B)
        } while (m2);
    }
    STAMP(4);
}

extern "C" void kernel_launch(void* const* d_in, const int* in_sizes, int n_in, void* d_out, int out_size, void* d_ws, size_t ws_size,
                              hipStream_t stream) {
    const u16* sv = (const u16*)d_in[0];
    const u16* pos = (const u16*)d_in[1];
    const int* src = (const int*)d_in[2];
    const int* dst = (const int*)d_in[3];
    const u16* freqs = (const u16*)d_in[4];
    const u16* w1 = (const u16*)d_in[5];
    const u16* w2 = (const u16*)d_in[6];
    const u16* wr = (const u16*)d_in[7];
    const int N = in_sizes[0] / 512, E = in_sizes[2];
    const int P = (N + NB - 1) / NB;
    const int chmax = std::min(CHMAX, ((LDS_BYTES - POS_BYTES - 4 * P - 64) / 10) & ~3);
    int nfilt = NFILT_MIN;
    while ((((E + nfilt - 1) / nfilt + 3) & ~3) > chmax) ++nfilt;
    const int chunk = ((E + nfilt - 1) / nfilt + 3) & ~3;
    char* ws = (char*)d_ws;
    size_t off = 0;
    auto take = [&](size_t bytes) { char* p = ws + off; off += (bytes + 255) / 256 * 256; return p; };
    char* tmain = take((size_t)N * 1024);
    char* tz = take((size_t)N * 256);
    char* rv = take((size_t)N * 1024);
    u4* cells = (u4*)take((size_t)nfilt * P * CAPC * 16);
    u32* cnts = (u32*)take((size_t)nfilt * P * 4);
    u4* ovf = (u4*)take((size_t)nfilt * chunk * 16);
    u32* ovfp = (u32*)take((size_t)nfilt * chunk * 4);
    u32* ovfn = (u32*)take((size_t)nfilt * 4);
    const int ntiles = (N + TM - 1) / TM;
    k1_kernel<<<nfilt + ntiles, K1T, 0, stream>>>(sv, pos, src, dst, w1, w2, tmain, tz, rv, cells, cnts, ovf, ovfp, ovfn, N, E, nfilt, chunk, P);
    constexpr int NP = 8 / S;
    const int nj = (P + NP - 1) / NP;
    k2_kernel<<<8 * nj, K2T, 0, stream>>>(sv, freqs, wr, cells, cnts, ovf, ovfp, ovfn, tmain, tz, rv, (u16*)d_out, N, nfilt, chunk, P);
}
```

```cpp
#include <hip/hip_runtime.h>
#include <stdint.h>
#include <algorithm>

#define NSLICE 2
#define ST_AUX 16
#define OUT_AUX 16
#define CAPC_ 12
#define DETERMINISTIC 1

typedef unsigned short u16;
typedef unsigned int u32;
typedef short s4 __attribute__((ext_vector_type(4)));
typedef short s8 __attribute__((ext_vector_type(8)));
typedef __bf16 bf2 __attribute__((ext_vector_type(2)));
typedef __bf16 bf8 __attribute__((ext_vector_type(8)));
typedef float f2 __attribute__((ext_vector_type(2)));
typedef float f4 __attribute__((ext_vector_type(4)));
typedef float f16v __attribute__((ext_vector_type(16)));
typedef u32 u2 __attribute__((ext_vector_type(2)));
typedef u32 u4 __attribute__((ext_vector_type(4)));
typedef __attribute__((address_space(3))) s4* lds_s4p;
typedef __attribute__((address_space(3))) void* lds_vp;

constexpr int S = NSLICE;
constexpr int G = 64 / S;
#define NBV 98
#define K2TV 1024
constexpr int NB = NBV;
constexpr int CAPC = (NB <= 32) ? 16 : 32;
#define RVMIN 4
constexpr bool USE_RV = (S >= RVMIN);
constexpr int TM = 64;
constexpr int K1T = 512;
constexpr int NFILT_MIN = 60;
constexpr int EPT = 16;
constexpr int CHMAX = K1T * EPT;
constexpr int LDS_BYTES = 163840;
constexpr int OFF_W2 = 0, OFF_W1 = 98304, OFF_S = 131072, OFF_H = 147456, OFF_V = 98304;
constexpr int RPITCH = 1296;
constexpr int POS_BYTES = 81920;
constexpr float CUTOFF = 5.0f;
constexpr int K2T = K2TV;
constexpr int K2W = K2T / 64;
constexpr int TCAP = (NB <= 32) ? 320 : (NB <= 64 ? 512 : 832);
constexpr int CAPN = (S >= 4) ? 32 : 48;
constexpr u32 OOB = 0x80000000u;
constexpr u32 INV = 0xffffffffu;
constexpr u32 MORE = 0xfffffffeu;

__device__ __forceinline__ float lo2f(u32 x) { return __uint_as_float(x << 16); }
__device__ __forceinline__ float hi2f(u32 x) { return __uint_as_float(x & 0xffff0000u); }
__device__ __forceinline__ u32 pack2(float a, float b) {
    bf2 r = {(__bf16)a, (__bf16)b};
    return __builtin_bit_cast(u32, r);
}
__device__ __forceinline__ void dma16(const void* g, void* l) {
    __builtin_amdgcn_global_load_lds((const u32*)g, (u32*)l, 16, 0, 0);
}
__device__ __forceinline__ void dma16_hidden(const void* g, void* l) {
    unsigned keep;
    const unsigned dstl = __builtin_amdgcn_readfirstlane((unsigned)(size_t)(__attribute__((address_space(3))) char*)l);
    asm volatile("s_mov_b32 %0, m0\n\ts_mov_b32 m0, %2\n\ts_nop 0\n\tglobal_load_lds_dwordx4 %1, off\n\ts_mov_b32 m0, %0"
                 : "=&s"(keep) : "v"(g), "s"(dstl) : "memory");
}
__device__ __forceinline__ s4 tr_read(const char* p) {
    return __builtin_amdgcn_ds_read_tr16_b64_v4i16((lds_s4p)(lds_vp)(p));
}
__device__ __forceinline__ bf8 cat8(s4 a, s4 b) {
    s8 r = __builtin_shufflevector(a, b, 0, 1, 2, 3, 4, 5, 6, 7);
    return __builtin_bit_cast(bf8, r);
}
__device__ __forceinline__ u32 mbcnt64(unsigned long long m) {
    return __builtin_amdgcn_mbcnt_hi((u32)(m >> 32), __builtin_amdgcn_mbcnt_lo((u32)m, 0));
}
__device__ __forceinline__ int wave_incl_scan(int v) {
    v += __builtin_amdgcn_update_dpp(0, v, 0x111, 0xf, 0xf, false);
    v += __builtin_amdgcn_update_dpp(0, v, 0x112, 0xf, 0xf, false);
    v += __builtin_amdgcn_update_dpp(0, v, 0x114, 0xf, 0xf, false);
    v += __builtin_amdgcn_update_dpp(0, v, 0x118, 0xf, 0xf, false);
    v += __builtin_amdgcn_update_dpp(0, v, 0x142, 0xa, 0xf, false);
    v += __builtin_amdgcn_update_dpp(0, v, 0x143, 0xc, 0xf, false);
    return v;
}
__device__ __forceinline__ void lds_barrier() {
    asm volatile("s_waitcnt lgkmcnt(0)" ::: "memory");
    __builtin_amdgcn_s_barrier();
    asm volatile("" ::: "memory");
}

#define STAMP(idx)
__device__ __forceinline__ void gemm_role(char* lds, int tile, const u16* __restrict__ sv, const u16* __restrict__ w1,
                                          const u16* __restrict__ w2, char* __restrict__ tmain, char* __restrict__ tz,
                                          char* __restrict__ rv, int N) {
    const int tid = threadIdx.x, lane = tid & 63, w = __builtin_amdgcn_readfirstlane(tid >> 6);
    const int n0 = tile * TM;
    const int stamp_base = 0;
    STAMP(0);
    const int l4 = lane >> 4, l15 = lane & 15;
#pragma unroll
    for (int i = 0; i < 2; ++i) {
        const int row = (2 * w + i) * 4 + l4, c = l15 ^ (row & 15);
        const int node = min(n0 + row, N - 1);
        dma16((const char*)sv + (size_t)node * 1024 + c * 16, lds + OFF_S + (2 * w + i) * 1024);
    }
    const u32 csw = (((l15 >> 2) ^ l4) << 6) | ((l15 & 3) << 4);
    {
        const char* g1 = (const char*)w1 + l4 * 256 + csw;
#pragma unroll
        for (int i = 0; i < 4; ++i) dma16(g1 + (4 * w + i) * 1024, lds + OFF_W1 + (4 * w + i) * 1024);
        const char* g2 = (const char*)w2 + l4 * 768 + csw;
#pragma unroll
        for (int t = 0; t < 3; ++t)
#pragma unroll
            for (int i = 0; i < 4; ++i) dma16(g2 + (4 * w + i) * 3072 + t * 256, lds + OFF_W2 + t * 32768 + (4 * w + i) * 1024);
    }
    __builtin_amdgcn_sched_barrier(0);
    u32 vreg[8][4];
    if (USE_RV) {
#pragma unroll
        for (int i = 0; i < 8; ++i) {
            const int node = min(n0 + 8 * w + i, N - 1);
            const char* p = (const char*)sv + (size_t)node * 1024;
#pragma unroll
            for (int q = 0; q < 4; ++q) {
                u32 t;
                asm volatile("global_load_dword %0, %1, %2 offset:%3" : "=v"(t) : "v"(lane * 4), "s"(p), "i"(q * 256) : "memory");
                vreg[i][q] = t;
            }
        }
    }
    __builtin_amdgcn_sched_barrier(0);
    if (USE_RV) asm volatile("s_waitcnt vmcnt(44)" ::: "memory"); else asm volatile("s_waitcnt vmcnt(12)" ::: "memory");
    __builtin_amdgcn_s_barrier();
    __builtin_amdgcn_sched_barrier(0);
    STAMP(1);

    const int r = lane & 31, h = lane >> 5, g = lane >> 4, tq = (lane & 15) >> 2, tp = lane & 3;
    const int nt = w & 1;
    const int row = 32 * nt + r;
    const int xsw = (h ^ (r & 15)) << 4;
    const int trl = (8 * (g >> 1) + tq) * 256 + 32 * (g & 1) + 8 * tp;
    {
        const int ht = w >> 1;
        const char* abase = lds + OFF_W1 + trl + ((ht ^ tq) << 6);
        const char* bbase = lds + OFF_S + row * 256;
        f16v acc = {};
#pragma unroll
        for (int ks = 0; ks < 8; ++ks) {
            s4 a0 = tr_read(abase + ks * 4096), a1 = tr_read(abase + ks * 4096 + 1024);
            bf8 b = *(const bf8*)(bbase + ((ks * 32) ^ xsw));
            acc = __builtin_amdgcn_mfma_f32_32x32x16_bf16(cat8(a0, a1), b, acc, 0, 0, 0);
        }
        char* hbase = lds + OFF_H + row * 256 + 8 * h;
#pragma unroll
        for (int gg = 0; gg < 4; ++gg) {
            float y[4];
#pragma unroll
            for (int j = 0; j < 4; ++j) { float a = acc[4 * gg + j]; y[j] = a * __builtin_amdgcn_rcpf(1.0f + __expf(-a)); }
            u2 o = {pack2(y[0], y[1]), pack2(y[2], y[3])};
            *(u2*)(hbase + (((4 * ht + gg) ^ (r & 15)) << 4)) = o;
        }
    }
    STAMP(2);
    asm volatile("s_waitcnt vmcnt(0) lgkmcnt(0)" ::: "memory");
    __builtin_amdgcn_s_barrier();
    __builtin_amdgcn_sched_barrier(0);
    STAMP(3);
#pragma unroll
    for (int i = 0; i < 6; ++i) {
        const int rr = (6 * w + i) * 4 + l4;
        const int nl = rr / 3, q = rr - nl * 3;
        const int node = min(n0 + nl, N - 1);
        dma16((const char*)sv + (size_t)node * 1024 + (q + 1) * 256 + ((l15 ^ (nl & 15)) << 4), lds + OFF_V + (6 * w + i) * 1024);
    }
    if (USE_RV) {
        auto rr = __builtin_amdgcn_make_buffer_rsrc((void*)rv, 0, (u32)N * 1024u, 0x00020000);
        const int sl = lane / G, gl = lane % G;
        const u32 lo = (u32)sl * N * (16 * G) + gl * 4;
#pragma unroll
        for (int i = 0; i < 8; ++i) {
            const int node = n0 + 8 * w + i;
            const u32 lo_ = (node < N) ? lo : OOB;
#pragma unroll
            for (int q = 0; q < 4; ++q) __builtin_amdgcn_raw_buffer_store_b32(vreg[i][q], rr, lo_ + q * (4 * G), min(node, N - 1) * (16 * G), ST_AUX);
        }
    }
    f16v acc2[3] = {};
    const int cb = w >> 1;
    {
        const char* bbase = lds + OFF_H + row * 256;
        const char* ab = lds + OFF_W2 + trl + ((cb ^ tq) << 6);
#pragma unroll
        for (int ks = 0; ks < 8; ++ks) {
            bf8 b = *(const bf8*)(bbase + ((ks * 32) ^ xsw));
#pragma unroll
            for (int t = 0; t < 3; ++t) {
                s4 a0 = tr_read(ab + t * 32768 + ks * 4096), a1 = tr_read(ab + t * 32768 + ks * 4096 + 1024);
                acc2[t] = __builtin_amdgcn_mfma_f32_32x32x16_bf16(cat8(a0, a1), b, acc2[t], 0, 0, 0);
            }
        }
    }
    if (USE_RV) asm volatile("s_waitcnt vmcnt(32) lgkmcnt(0)" ::: "memory"); else asm volatile("s_waitcnt vmcnt(0) lgkmcnt(0)" ::: "memory");
    __builtin_amdgcn_s_barrier();
    __builtin_amdgcn_sched_barrier(0);
    STAMP(4);
    {
        char* rrow = lds + row * RPITCH;
        const char* vrow = lds + OFF_V + row * 768 + 8 * h;
#pragma unroll
        for (int gg = 0; gg < 4; ++gg) {
            const int csw2 = ((4 * cb + gg) ^ (r & 15)) << 4;
            const u2 vx = *(const u2*)(vrow + csw2), vy = *(const u2*)(vrow + 256 + csw2), vz = *(const u2*)(vrow + 512 + csw2);
            const float a0 = acc2[1][4 * gg], a1 = acc2[1][4 * gg + 1], a2 = acc2[1][4 * gg + 2], a3 = acc2[1][4 * gg + 3];
            const int c0 = 32 * cb + 8 * gg + 4 * h;
            u4 o0 = {pack2(acc2[0][4 * gg], acc2[0][4 * gg + 1]), pack2(acc2[2][4 * gg], acc2[2][4 * gg + 1]),
                     pack2(a0 * lo2f(vx[0]), a1 * hi2f(vx[0])), pack2(a0 * lo2f(vy[0]), a1 * hi2f(vy[0]))};
            u4 o1 = {pack2(acc2[0][4 * gg + 2], acc2[0][4 * gg + 3]), pack2(acc2[2][4 * gg + 2], acc2[2][4 * gg + 3]),
                     pack2(a2 * lo2f(vx[1]), a3 * hi2f(vx[1])), pack2(a2 * lo2f(vy[1]), a3 * hi2f(vy[1]))};
            u2 oz = {pack2(a0 * lo2f(vz[0]), a1 * hi2f(vz[0])), pack2(a2 * lo2f(vz[1]), a3 * hi2f(vz[1]))};
            *(u4*)(rrow + c0 * 8) = o0;
            *(u4*)(rrow + c0 * 8 + 16) = o1;
            *(u2*)(rrow + 1024 + c0 * 2) = oz;
        }
    }
    lds_barrier();
    __builtin_amdgcn_sched_barrier(0);
    STAMP(5);
    {
        auto rm = __builtin_amdgcn_make_buffer_rsrc((void*)tmain, 0, (u32)N * 1024u, 0x00020000);
        const int gq = lane / G, gl = lane % G;
#pragma unroll
        for (int i = 0; i < 8; ++i) {
            const int p = w * 8 + i;
            const int sl = p % S, nb = (p / S) * S;
            const int nn = nb + gq;
            u4 v = *(const u4*)(lds + nn * RPITCH + (sl * G + gl) * 16);
            const int node = n0 + nn;
            u32 off = (node < N) ? ((u32)(sl * N + node) * G + gl) * 16u : OOB;
            __builtin_amdgcn_raw_buffer_store_b128(v, rm, off, 0, ST_AUX);
        }
        auto rz = __builtin_amdgcn_make_buffer_rsrc((void*)tz, 0, (u32)N * 256u, 0x00020000);
#pragma unroll
        for (int i = 0; i < 2; ++i) {
            const int q = (w * 2 + i) * 64 + lane;
            constexpr int CPN = G / 4;
            const int sl = q / (64 * CPN), rem = q % (64 * CPN), nn = rem / CPN, ck = rem % CPN;
            u4 v = *(const u4*)(lds + nn * RPITCH + 1024 + (sl * G + ck * 4) * 4);
            const int node = n0 + nn;
            u32 off = (node < N) ? ((u32)(sl * N + node) * G + ck * 4) * 4u : OOB;
            __builtin_amdgcn_raw_buffer_store_b128(v, rz, off, 0, ST_AUX);
        }
    }
    STAMP(6);
}

template <bool POS_LDS>
__device__ __forceinline__ void get_pos(const char* lds, const u16* __restrict__ pos, u32 n, float& x, float& y, float& z) {
    if (POS_LDS) {
        const u32* pp = (const u32*)(lds + ((n * 6u) & ~3u));
        const u32 d0 = pp[0], d1 = pp[1];
        const u32 sh = n << 4;
        const u32 xy = __builtin_amdgcn_alignbit(d1, d0, sh);
        const u32 zz = d1 >> (sh & 31);
        x = lo2f(xy); y = hi2f(xy); z = lo2f(zz);
    } else {
        x = __uint_as_float((u32)pos[3 * n] << 16);
        y = __uint_as_float((u32)pos[3 * n + 1] << 16);
        z = __uint_as_float((u32)pos[3 * n + 2] << 16);
    }
}

template <bool POS_LDS>
__device__ __forceinline__ void filter_role(char* lds, int f, int chunk, const u16* __restrict__ pos, const int* __restrict__ src,
                                            const int* __restrict__ dst, u4* __restrict__ cells, u32* __restrict__ cnts, u4* __restrict__ ovf,
                                            u32* __restrict__ ovfp, u32* __restrict__ ovfn, int N, int E, int P) {
    const int tid = threadIdx.x, lane = tid & 63, w = __builtin_amdgcn_readfirstlane(tid >> 6);
    const int stamp_base = 0;
    STAMP(0);
    const int e0 = f * chunk, n = min(E, e0 + chunk) - e0;
    u32* eS = (u32*)(lds + POS_BYTES);
    u32* eD = eS + chunk;
    u16* queue = (u16*)(eD + chunk);
    u32* ocnt = (u32*)(lds + POS_BYTES + chunk * 10);
    u32* hist = ocnt + 4;
    {
        const int n4 = n >> 2;
        for (int q0 = w * 64; q0 < n4; q0 += K1T) {
            const int q = q0 + lane;
            if (q < n4) { dma16(src + e0 + q * 4, (char*)eS + q0 * 16); dma16(dst + e0 + q * 4, (char*)eD + q0 * 16); }
        }
        if (tid < 4) {
            const int e = n4 * 4 + tid;
            if (e < chunk) { eS[e] = e < n ? src[e0 + e] : 0; eD[e] = e < n ? dst[e0 + e] : 0; }
        }
    }
    if (POS_LDS) {
        const int bytes = N * 6, nch = bytes >> 4;
        for (int p = w; p * 64 < nch; p += 8) {
            int q = p * 64 + lane;
            if (q < nch) dma16((const char*)pos + q * 16, lds + p * 1024);
        }
        const int rem = (bytes - (nch << 4)) >> 1;
        if (tid < rem) ((u16*)lds)[nch * 8 + tid] = pos[nch * 8 + tid];
    }
    for (int i = tid; i < P; i += K1T) hist[i] = 0;
    if (tid == 0) { ocnt[0] = 0; ocnt[1] = 0; }
    asm volatile("s_waitcnt vmcnt(0) lgkmcnt(0)" ::: "memory");
    __builtin_amdgcn_s_barrier();
    asm volatile("" ::: "memory");
    STAMP(1);
    u32 actmask = 0;
#pragma unroll
    for (int it = 0; it < 4; ++it) {
        const int e = (it * K1T + tid) * 4;
        if (e < n) {
            const u4 a = *(const u4*)(eS + e), b = *(const u4*)(eD + e);
            u32 m4 = 0;
#pragma unroll
            for (int j = 0; j < 4; ++j) {
                float ax, ay, az, bx, by, bz;
                get_pos<POS_LDS>(lds, pos, a[j], ax, ay, az);
                get_pos<POS_LDS>(lds, pos, b[j], bx, by, bz);
                const float rx = ax - bx, ry = ay - by, rz = az - bz;
                const float d2 = __builtin_fmaf(rz, rz, __builtin_fmaf(ry, ry, __builtin_fmaf(rx, rx, 1e-8f)));
                m4 |= (d2 >= CUTOFF * CUTOFF && d2 <= 3.0e38f) ? 0u : (1u << j);
            }
            if (n - e < 4) m4 &= (1u << (n - e)) - 1u;
            actmask |= m4 << (it * 4);
        }
    }
    STAMP(2);
    {
        const int cntt = __popc(actmask);
        const int inc = wave_incl_scan(cntt);
        u32 qb = 0;
        if (lane == 63) qb = atomicAdd(&ocnt[1], (u32)inc);
        qb = __builtin_amdgcn_readlane(qb, 63) + (inc - cntt);
        u32 m = actmask;
        while (m) {
            const int bit = __builtin_ctz(m); m &= m - 1;
            queue[qb++] = (u16)((((bit >> 2) * K1T + tid) << 2) + (bit & 3));
        }
    }
    lds_barrier();
    STAMP(3);
    const int qn = ocnt[1];
    u4* cl = cells + (size_t)f * P * CAPC;
    for (int j = tid; j < qn; j += K1T) {
        const int idx = queue[j];
        const u32 s = eS[idx], dd = eD[idx];
        const u32 p = dd / NB;
        const u32 rank = atomicAdd(&hist[p], 1u);
        float ax, ay, az, bx, by, bz;
        get_pos<POS_LDS>(lds, pos, s, ax, ay, az);
        get_pos<POS_LDS>(lds, pos, dd, bx, by, bz);
        const u4 r = {s | ((dd - p * NB) << 24), __float_as_uint(ax - bx), __float_as_uint(ay - by), __float_as_uint(az - bz)};
        if (rank < CAPC) cl[p * CAPC + rank] = r;
        else { const u32 o = atomicAdd(&ocnt[0], 1u); ovf[(size_t)f * chunk + o] = r; ovfp[(size_t)f * chunk + o] = p; }
    }
    lds_barrier();
    for (int p = tid; p < P; p += K1T) cnts[(size_t)f * P + p] = hist[p];
    if (tid == 0) ovfn[f] = ocnt[0];
    STAMP(4);
}

__global__ __launch_bounds__(K1T) void k1_kernel(const u16* __restrict__ sv, const u16* __restrict__ pos, const int* __restrict__ src,
                                                 const int* __restrict__ dst, const u16* __restrict__ w1, const u16* __restrict__ w2,
                                                 char* __restrict__ tmain, char* __restrict__ tz, char* __restrict__ rv,
                                                 u4* __restrict__ cells, u32* __restrict__ cnts, u4* __restrict__ ovf, u32* __restrict__ ovfp, u32* __restrict__ ovfn,
                                                 int N, int E, int nfilt, int chunk, int P) {
    __shared__ __attribute__((aligned(1024))) char lds[LDS_BYTES];
    const int b = blockIdx.x;
    if (b < nfilt) {
        if (N * 6 + 16 <= POS_BYTES) filter_role<true>(lds, b, chunk, pos, src, dst, cells, cnts, ovf, ovfp, ovfn, N, E, P);
        else filter_role<false>(lds, b, chunk, pos, src, dst, cells, cnts, ovf, ovfp, ovfn, N, E, P);
    } else {
        gemm_role(lds, b - nfilt, sv, w1, w2, tmain, tz, rv, N);
    }
}

template <int I> __device__ __forceinline__ u32 qb(u32 x) {
    return __builtin_amdgcn_update_dpp(0u, x, I | (I << 2) | (I << 4) | (I << 6), 0xf, 0xf, true);
}
template <int I> __device__ __forceinline__ float qbf(u32 x) { return __uint_as_float(qb<I>(x)); }
__device__ __forceinline__ float fast_sin_rev(float rev) { return __builtin_amdgcn_sinf(__builtin_amdgcn_fractf(rev)); }
__device__ __forceinline__ float fast_cos_rev(float rev) { return __builtin_amdgcn_cosf(__builtin_amdgcn_fractf(rev)); }

struct Buf { u4 d[4]; u32 z[4]; u4 rb; u32 pn; };

__global__ __launch_bounds__(K2T, 4) void k2_kernel(const u16* __restrict__ sv, const u16* __restrict__ freqs, const u16* __restrict__ wr,
                                                    const u4* __restrict__ cells, const u32* __restrict__ cnts, const u4* __restrict__ ovf,
                                                    const u32* __restrict__ ovfp, const u32* __restrict__ ovfn,
                                                    const char* __restrict__ tmain, const char* __restrict__ tz, const char* __restrict__ rv,
                                                    u16* __restrict__ out, int N, int C, int chunk, int P) {
    __shared__ u4 recsA[TCAP + 1];
    __shared__ u4 recsB[TCAP + 1];
    __shared__ u32 keys[NB * CAPN];
    __shared__ u16 Lx[NB * CAPN];
    __shared__ u16 slotL[TCAP];
    __shared__ __attribute__((aligned(16))) int ncnt[(NB + 3) / 4 * 4];
    __shared__ unsigned char order[NB];
    __shared__ int Tsh, tetctr, slow, slow2, novl, ovl[8];
    __shared__ __attribute__((aligned(16))) u32 resL[K2W][2][256];
    const int tid = threadIdx.x, lane = tid & 63, w = __builtin_amdgcn_readfirstlane(tid >> 6);
    const int stamp_base = 512;
    STAMP(0);
    constexpr int NP = 8 / S;
    const int x = blockIdx.x & 7, j = blockIdx.x >> 3;
    const int slice = x % S, part = x / S;
    const int p = j * NP + part;
    if (p >= P) return;
    const int n0 = p * NB;
    const int grp = lane / G, gl = lane % G;
    const int ch = slice * (128 / S) + 2 * gl;
    auto rm = __builtin_amdgcn_make_buffer_rsrc((void*)(tmain + (size_t)slice * N * (G * 16)), 0, (u32)N * (G * 16), 0x00020000);
    auto rz = __builtin_amdgcn_make_buffer_rsrc((void*)(tz + (size_t)slice * N * (G * 4)), 0, (u32)N * (G * 4), 0x00020000);
    auto ro = __builtin_amdgcn_make_buffer_rsrc((void*)out, 0, (u32)N * 1024u, 0x00020000);

    constexpr int RPT = (64 * CAPC + K2T - 1) / K2T;
    const int nslots = C * CAPC;
    const u32 cstride = (u32)P * CAPC * 16;
    auto rc = __builtin_amdgcn_make_buffer_rsrc((void*)(cells + (size_t)p * CAPC), 0, (u32)C * cstride - (u32)p * CAPC * 16, 0x00020000);
    auto rn = __builtin_amdgcn_make_buffer_rsrc((void*)(cnts + p), 0, ((u32)C * P - p) * 4, 0x00020000);
    u4 raw[RPT]; u32 rcnt[RPT];
#pragma unroll
    for (int k = 0; k < RPT; ++k) {
        const u32 q = tid + k * K2T, c = q / CAPC;
        raw[k] = __builtin_amdgcn_raw_buffer_load_b128(rc, c * cstride + (q % CAPC) * 16, 0, 0);
        rcnt[k] = __builtin_amdgcn_raw_buffer_load_b32(rn, c * (u32)P * 4, 0, 0);
    }
    const u32 fq = freqs[lane & 3];
    __builtin_amdgcn_sched_barrier(0);
    if (tid < (NB + 3) / 4 * 4) ncnt[tid] = (tid < NB) ? 0 : -1;
    if (tid == 64) { recsA[TCAP] = u4{0, 0, 0, 0}; recsB[TCAP] = u4{OOB, 0, 0, 0}; Tsh = 0; tetctr = K2W; slow2 = 0; slow = (nslots > RPT * K2T) ? 1 : 0; novl = 0; }
    u32 wk[3][4];
#pragma unroll
    for (int t = 0; t < 3; ++t)
#pragma unroll
        for (int k = 0; k < 4; ++k) wk[t][k] = *(const u32*)(wr + k * 384 + t * 128 + ch);
    lds_barrier();
    STAMP(1);
    auto push_raw = [&](const u4& r, bool valid) {
        const unsigned long long m = __ballot(valid);
        u32 ub = 0;
        if (lane == 0) ub = atomicAdd(&Tsh, (int)__popcll(m));
        const u32 u = __builtin_amdgcn_readfirstlane(ub) + mbcnt64(m);
        if (valid && u < TCAP) recsB[u] = r;
    };
#pragma unroll
    for (int k = 0; k < RPT; ++k) {
        const u32 q = tid + k * K2T;
        if (rcnt[k] > CAPC && (q % CAPC) == 0) { const int o = atomicAdd(&novl, 1); if (o < 8) ovl[o] = q / CAPC; else slow = 1; }
        push_raw(raw[k], (q % CAPC) < rcnt[k]);
    }
    lds_barrier();
    if (slow | novl) {
        if (slow) {
            lds_barrier();
            if (tid == 0) Tsh = TCAP + 1;
        } else {
            const int no_ = novl;
            for (int oi = 0; oi < no_; ++oi) {
                const int c = ovl[oi], no = ovfn[c];
                for (int i0 = 0; i0 < no; i0 += K2T) {
                    const int i = min(i0 + tid, no - 1);
                    const u32 pp = ovfp[(size_t)c * chunk + i];
                    const u4 r = ovf[(size_t)c * chunk + i];
                    push_raw(r, i0 + tid < no && pp == (u32)p);
                }
            }
        }
        lds_barrier();
    }
    float fr[4];
#pragma unroll
    for (int k = 0; k < 4; ++k) fr[k] = __uint_as_float(__builtin_amdgcn_readlane(fq, k) << 16) * 0.15915494309189535f;
    const int T = Tsh;
    for (int u = tid; u < min(T, TCAP); u += K2T) {
        const u4 r = recsB[u];
        const int dl = r[0] >> 24;
        const int sl = atomicAdd(&ncnt[dl], 1);
        const u32 key = (r[0] & 0xffffffu) * (G * 16);
        if (sl < CAPN) keys[dl * CAPN + sl] = key; else slow2 = 1;
        slotL[u] = (u16)((dl << 8) | (sl & 255));
        const float rx = __uint_as_float(r[1]), ry = __uint_as_float(r[2]), rz_ = __uint_as_float(r[3]);
        const float d = __builtin_amdgcn_sqrtf(__builtin_fmaf(rz_, rz_, __builtin_fmaf(ry, ry, __builtin_fmaf(rx, rx, 1e-8f))));
        const float inv = __builtin_amdgcn_rcpf(d);
        const float fc = (d < CUTOFF) ? 0.5f * fast_cos_rev(d * (0.5f / CUTOFF)) + 0.5f : 0.0f * d;
        const float sc = inv * fc;
        float gk[4];
#pragma unroll
        for (int q = 0; q < 4; ++q) gk[q] = fast_sin_rev(d * fr[q]) * sc;
        const u32 h01 = pack2(gk[0], gk[1]), h23 = pack2(gk[2], gk[3]);
        const u32 l01 = pack2(gk[0] - lo2f(h01), gk[1] - hi2f(h01)), l23 = pack2(gk[2] - lo2f(h23), gk[3] - hi2f(h23));
        recsA[u] = u4{h01, h23, l01, l23};
        recsB[u] = u4{key, __float_as_uint(rx * inv), __float_as_uint(ry * inv), __float_as_uint(rz_ * inv)};
    }
    s4 wb[3][2];
#pragma unroll
    for (int t = 0; t < 3; ++t) {
        u2 b0 = {__builtin_amdgcn_perm(wk[t][1], wk[t][0], 0x05040100), __builtin_amdgcn_perm(wk[t][3], wk[t][2], 0x05040100)};
        u2 b1 = {__builtin_amdgcn_perm(wk[t][1], wk[t][0], 0x07060302), __builtin_amdgcn_perm(wk[t][3], wk[t][2], 0x07060302)};
        wb[t][0] = __builtin_bit_cast(s4, b0);
        wb[t][1] = __builtin_bit_cast(s4, b1);
    }
    lds_barrier();
    STAMP(2);
    if (T > TCAP || slow2) {
        float frs[4];
#pragma unroll
        for (int k = 0; k < 4; ++k) frs[k] = __uint_as_float((u32)freqs[k] << 16);
        for (int tet = w; tet < NB / S; tet += K2W) {
            const int nl = tet * S + grp, node = n0 + nl;
            float acc[4][2] = {};
            for (int c = 0; c < 2 * C; ++c) {
                const int cc = c < C ? c : c - C;
                const u32 cn = cnts[(size_t)cc * P + p];
                const int l = c < C ? (int)min(cn, (u32)CAPC) : (cn > CAPC ? (int)ovfn[cc] : 0);
                for (int base = 0; base < l; base += 64) {
                    u4 rr = {INV, 0, 0, 0};
                    bool ok = base + lane < l;
                    if (ok) {
                        if (c < C) rr = cells[((size_t)cc * P + p) * CAPC + base + lane];
                        else if (ovfp[(size_t)cc * chunk + base + lane] == (u32)p) rr = ovf[(size_t)cc * chunk + base + lane];
                        else ok = false;
                    }
                    const int dl = rr[0] >> 24;
                    unsigned long long m = __ballot(ok && (dl / S == tet));
                    while (m) {
                        const int i = __builtin_ctzll(m); m &= m - 1;
                        const u32 q0 = __builtin_amdgcn_readlane(rr[0], i);
                        const float rx = __uint_as_float(__builtin_amdgcn_readlane(rr[1], i)), ry = __uint_as_float(__builtin_amdgcn_readlane(rr[2], i)),
                                    rz_ = __uint_as_float(__builtin_amdgcn_readlane(rr[3], i));
                        const float d = sqrtf(rx * rx + ry * ry + rz_ * rz_ + 1e-8f), inv = 1.0f / d;
                        const float fc = (d < CUTOFF) ? 0.5f * (cosf(3.14159265358979323846f * d / CUTOFF) + 1.0f) : 0.0f * d;
                        float W[3][2] = {};
#pragma unroll 1
                        for (int k = 0; k < 4; ++k) {
                            const float fk = k == 0 ? frs[0] : (k == 1 ? frs[1] : (k == 2 ? frs[2] : frs[3]));
                            const float gk = sinf(d * fk) * inv * fc;
#pragma unroll
                            for (int t = 0; t < 3; ++t) {
                                const u32 wv = k == 0 ? wk[t][0] : (k == 1 ? wk[t][1] : (k == 2 ? wk[t][2] : wk[t][3]));
                                W[t][0] += gk * lo2f(wv); W[t][1] += gk * hi2f(wv);
                            }
                        }
                        if ((int)(q0 >> 24) == nl) {
                            const u32 off = ((q0 & 0xffffffu) * G + gl) * 4u;
                            const u4 dd = __builtin_amdgcn_raw_buffer_load_b128(rm, off * 4u, 0, 0);
                            const u32 zz = __builtin_amdgcn_raw_buffer_load_b32(rz, off, 0, 0);
                            acc[0][0] += W[0][0] * lo2f(dd[0]); acc[0][1] += W[0][1] * hi2f(dd[0]);
                            const float t0 = W[2][0] * lo2f(dd[1]), t1 = W[2][1] * hi2f(dd[1]);
                            acc[1][0] += W[1][0] * lo2f(dd[2]) + t0 * rx * inv; acc[1][1] += W[1][1] * hi2f(dd[2]) + t1 * rx * inv;
                            acc[2][0] += W[1][0] * lo2f(dd[3]) + t0 * ry * inv; acc[2][1] += W[1][1] * hi2f(dd[3]) + t1 * ry * inv;
                            acc[3][0] += W[1][0] * lo2f(zz) + t0 * rz_ * inv;   acc[3][1] += W[1][1] * hi2f(zz) + t1 * rz_ * inv;
                        }
                    }
                }
            }
            if (node < N) {
#pragma unroll
                for (int q = 0; q < 4; ++q) {
                    const u32 r = *(const u32*)((const char*)sv + (size_t)node * 1024 + q * 256 + ch * 2);
                    *(u32*)((char*)out + (size_t)node * 1024 + q * 256 + ch * 2) = pack2(lo2f(r) + acc[q][0], hi2f(r) + acc[q][1]);
                }
            }
        }
        return;
    }
    if (tid >= K2T - 128 && tid - (K2T - 128) < NB) {
        const int n = tid - (K2T - 128), c = ncnt[n];
        int rk = 0;
        for (int i = 0; i < NB; i += 4) {
            typedef int i4 __attribute__((ext_vector_type(4)));
            const i4 q = *(const i4*)(ncnt + i);
#pragma unroll
            for (int k = 0; k < 4; ++k) rk += (q[k] > c || (q[k] == c && i + k < n)) ? 1 : 0;
        }
        order[rk] = (unsigned char)n;
    }
    for (int u = tid; u < T; u += K2T) {
        const int ds = slotL[u], dl = ds >> 8, sl = ds & 255;
        const int c = ncnt[dl];
        const u32 key = recsB[u][0];
        int rank = 0;
        for (int i = 0; i < c; ++i) { u32 ki = keys[dl * CAPN + i]; rank += (ki < key || (ki == key && i < sl)) ? 1 : 0; }
        Lx[dl * CAPN + rank] = (u16)u;
    }
    lds_barrier();
    STAMP(3);
    float as0 = 0, as1 = 0, ax0 = 0, ax1 = 0, ay0 = 0, ay1 = 0, az0 = 0, az1 = 0;
    const u32 gl16 = gl * 16;
    int i_tet = -1, i_b = 0, i_mm = 0, i_st = 0, i_c = 0, i_nl = 0, i_par = 0;
    auto advance = [&]() -> bool {
        i_b += 4;
        if (i_b < i_mm) return true;
        if (i_tet < 0) i_tet = w;
        else {
            int t = 0;
            if (lane == 0) t = atomicAdd(&tetctr, 1);
            i_tet = __builtin_amdgcn_readfirstlane(t);
        }
        if (i_tet >= NB / S) return false;
        const int nl = order[i_tet * S + grp];
        i_nl = nl; i_st = nl * CAPN; i_c = ncnt[nl];
        int mm = 1;
#pragma unroll
        for (int g2 = 0; g2 < S; ++g2) mm = max(mm, __builtin_amdgcn_readlane(i_c, g2 * G));
        i_mm = mm;
        i_b = 0;
        i_par ^= 1;
        {
            constexpr int CPR = G / 4;
            const int rem = lane % (4 * CPR);
            const int node = min(n0 + (int)order[i_tet * S + lane / (4 * CPR)], N - 1);
            const char* src_ = USE_RV ? rv + ((size_t)slice * N + node) * (16 * G) + rem * 16
                                      : (const char*)sv + (size_t)node * 1024 + (rem / CPR) * 256 + slice * (G * 4) + (rem % CPR) * 16;
            dma16_hidden(src_, (char*)resL[w][i_par]);
        }
        return true;
    };
#define ISSUE1(B, I) { \
        const u32 off_ = qb<I>(B.rb[0]) + gl16; \
        B.d[I] = __builtin_amdgcn_raw_buffer_load_b128(rm, off_, 0, 0); \
        B.z[I] = __builtin_amdgcn_raw_buffer_load_b32(rz, off_ >> 2, 0, 0); }
#define ISSUE(B, OK) { \
        const int k_ = i_b + (lane & 3); \
        int idx_ = Lx[min(i_st + k_, NB * CAPN - 1)]; \
        idx_ = ((OK) && k_ < i_c) ? idx_ : TCAP; \
        B.pn = (u32)idx_ | ((u32)i_nl << 16); \
        B.rb = recsB[idx_]; \
        ISSUE1(B, 0) ISSUE1(B, 1) ISSUE1(B, 2) ISSUE1(B, 3) \
        C_##B##_n = (OK) ? min(4, i_mm - i_b) : 0; C_##B##_par = i_par; }
#define OPQ(X) asm volatile("" : "+v"(X))
#define STEP(B, I) { \
        const float rx = qbf<I>(B.rb[1]), ry = qbf<I>(B.rb[2]), rz_ = qbf<I>(B.rb[3]); \
        const float t0 = Wr0[I] * lo2f(B.d[I][1]), t1 = Wr1[I] * hi2f(B.d[I][1]); \
        as0 = __builtin_fmaf(Ws0[I], lo2f(B.d[I][0]), as0); OPQ(as0); as1 = __builtin_fmaf(Ws1[I], hi2f(B.d[I][0]), as1); OPQ(as1); \
        ax0 = __builtin_fmaf(Wv0[I], lo2f(B.d[I][2]), ax0); OPQ(ax0); ax1 = __builtin_fmaf(Wv1[I], hi2f(B.d[I][2]), ax1); OPQ(ax1); \
        ay0 = __builtin_fmaf(Wv0[I], lo2f(B.d[I][3]), ay0); OPQ(ay0); ay1 = __builtin_fmaf(Wv1[I], hi2f(B.d[I][3]), ay1); OPQ(ay1); \
        az0 = __builtin_fmaf(Wv0[I], lo2f(B.z[I]), az0); OPQ(az0);    az1 = __builtin_fmaf(Wv1[I], hi2f(B.z[I]), az1); OPQ(az1); \
        ax0 = __builtin_fmaf(t0, rx, ax0); OPQ(ax0); ax1 = __builtin_fmaf(t1, rx, ax1); OPQ(ax1); \
        ay0 = __builtin_fmaf(t0, ry, ay0); OPQ(ay0); ay1 = __builtin_fmaf(t1, ry, ay1); OPQ(ay1); \
        az0 = __builtin_fmaf(t0, rz_, az0); OPQ(az0); az1 = __builtin_fmaf(t1, rz_, az1); OPQ(az1); }
#define WC(T, C2) __builtin_amdgcn_mfma_f32_4x4x4bf16_1k(ghi, wb[T][C2], __builtin_amdgcn_mfma_f32_4x4x4bf16_1k(glo, wb[T][C2], zf_, 0, 0, 0), 0, 0, 0)
#define COMPUTE(B) { \
        const f4 zf_ = {0, 0, 0, 0}; \
        const u4 ga_ = recsA[B.pn & 0xffffu]; \
        const u2 ghi_ = {ga_[0], ga_[1]}, glo_ = {ga_[2], ga_[3]}; \
        const s4 ghi = __builtin_bit_cast(s4, ghi_), glo = __builtin_bit_cast(s4, glo_); \
        const f4 Ws0 = WC(0, 0), Ws1 = WC(0, 1), Wv0 = WC(1, 0), Wv1 = WC(1, 1), Wr0 = WC(2, 0), Wr1 = WC(2, 1); \
        asm volatile("" :: "v"(B.d[0]), "v"(B.d[1]), "v"(B.d[2]), "v"(B.d[3]), "v"(B.z[0]), "v"(B.z[1]), "v"(B.z[2]), "v"(B.z[3])); \
        STEP(B, 0) \
        if (C_##B##_n > 1) STEP(B, 1) \
        if (C_##B##_n > 2) STEP(B, 2) \
        if (C_##B##_n > 3) STEP(B, 3) \
        if (C_##B##_fin) { \
            const int nl_ = B.pn >> 16, node_ = n0 + nl_; \
            { \
                const u32* rp = resL[w][C_##B##_par] + grp * (4 * G) + gl; \
                const u32 r0 = rp[0], r1 = rp[G], r2 = rp[2 * G], r3 = rp[3 * G]; \
                const u32 o = (node_ < N) ? (u32)node_ * 1024u + ch * 2 : OOB; \
                __builtin_amdgcn_raw_buffer_store_b32(pack2(lo2f(r0) + as0, hi2f(r0) + as1), ro, o, 0, OUT_AUX); \
                __builtin_amdgcn_raw_buffer_store_b32(pack2(lo2f(r1) + ax0, hi2f(r1) + ax1), ro, o, 256, OUT_AUX); \
                __builtin_amdgcn_raw_buffer_store_b32(pack2(lo2f(r2) + ay0, hi2f(r2) + ay1), ro, o, 512, OUT_AUX); \
                __builtin_amdgcn_raw_buffer_store_b32(pack2(lo2f(r3) + az0, hi2f(r3) + az1), ro, o, 768, OUT_AUX); \
            } \
            as0 = as1 = ax0 = ax1 = ay0 = ay1 = az0 = az1 = 0.f; \
        } }
    {
        Buf A, B;
        int C_A_n, C_B_n, C_A_par, C_B_par; bool C_A_fin, C_B_fin;
        (void)advance();
        ISSUE(A, true) C_A_fin = (i_b + 4 >= i_mm);
        bool m2;
        do {
            const bool m1 = advance();
            ISSUE(B, m1) C_B_fin = m1 && (i_b + 4 >= i_mm);
            COMPUTE(A)
            m2 = m1 && advance();
            ISSUE(A, m2) C_A_fin = m2 && (i_b + 4 >= i_mm);
            COMPUTE(B)
        } while (m2);
    }
    STAMP(4);
}

extern "C" void kernel_launch(void* const* d_in, const int* in_sizes, int n_in, void* d_out, int out_size, void* d_ws, size_t ws_size,
                              hipStream_t stream) {
    const u16* sv = (const u16*)d_in[0];
    const u16* pos = (const u16*)d_in[1];
    const int* src = (const int*)d_in[2];
    const int* dst = (const int*)d_in[3];
    const u16* freqs = (const u16*)d_in[4];
    const u16* w1 = (const u16*)d_in[5];
    const u16* w2 = (const u16*)d_in[6];
    const u16* wr = (const u16*)d_in[7];
    const int N = in_sizes[0] / 512, E = in_sizes[2];
    const int P = (N + NB - 1) / NB;
    const int chmax = std::min(CHMAX, ((LDS_BYTES - POS_BYTES - 4 * P - 64) / 10) & ~3);
    int nfilt = NFILT_MIN;
    while ((((E + nfilt - 1) / nfilt + 3) & ~3) > chmax) ++nfilt;
    const int chunk = ((E + nfilt - 1) / nfilt + 3) & ~3;
    char* ws = (char*)d_ws;
    size_t off = 0;
    auto take = [&](size_t bytes) { char* p = ws + off; off += (bytes + 255) / 256 * 256; return p; };
    char* tmain = take((size_t)N * 1024);
    char* tz = take((size_t)N * 256);
    char* rv = take((size_t)N * 1024);
    u4* cells = (u4*)take((size_t)nfilt * P * CAPC * 16);
    u32* cnts = (u32*)take((size_t)nfilt * P * 4);
    u4* ovf = (u4*)take((size_t)nfilt * chunk * 16);
    u32* ovfp = (u32*)take((size_t)nfilt * chunk * 4);
    u32* ovfn = (u32*)take((size_t)nfilt * 4);
    const int ntiles = (N + TM - 1) / TM;
    k1_kernel<<<nfilt + ntiles, K1T, 0, stream>>>(sv, pos, src, dst, w1, w2, tmain, tz, rv, cells, cnts, ovf, ovfp, ovfn, N, E, nfilt, chunk, P);
    constexpr int NP = 8 / S;
    const int nj = (P + NP - 1) / NP;
    k2_kernel<<<8 * nj, K2T, 0, stream>>>(sv, freqs, wr, cells, cnts, ovf, ovfp, ovfn, tmain, tz, rv, (u16*)d_out, N, nfilt, chunk, P);
}
```

```cpp
#include <hip/hip_runtime.h>
#include <stdint.h>
#include <algorithm>

#define NSLICE 2
#define ST_AUX 16
#define OUT_AUX 16
#define CAPC_ 12
#define DETERMINISTIC 1

typedef unsigned short u16;
typedef unsigned int u32;
typedef short s4 __attribute__((ext_vector_type(4)));
typedef short s8 __attribute__((ext_vector_type(8)));
typedef __bf16 bf2 __attribute__((ext_vector_type(2)));
typedef __bf16 bf8 __attribute__((ext_vector_type(8)));
typedef float f2 __attribute__((ext_vector_type(2)));
typedef float f4 __attribute__((ext_vector_type(4)));
typedef float f16v __attribute__((ext_vector_type(16)));
typedef u32 u2 __attribute__((ext_vector_type(2)));
typedef u32 u4 __attribute__((ext_vector_type(4)));
typedef __attribute__((address_space(3))) s4* lds_s4p;
typedef __attribute__((address_space(3))) void* lds_vp;

constexpr int S = NSLICE;
constexpr int G = 64 / S;
#define NBV 98
#define K2TV 1024
constexpr int NB = NBV;
constexpr int CAPC = (NB <= 32) ? 16 : 32;
#define RVMIN 4
constexpr bool USE_RV = (S >= RVMIN);
constexpr int TM = 64;
constexpr int K1T = 512;
constexpr int NFILT_MIN = 60;
constexpr int EPT = 16;
constexpr int CHMAX = K1T * EPT;
constexpr int LDS_BYTES = 163840;
constexpr int OFF_W2 = 0, OFF_W1 = 98304, OFF_S = 131072, OFF_H = 147456, OFF_V = 98304;
constexpr int RPITCH = 1296;
constexpr int POS_BYTES = 81920;
constexpr float CUTOFF = 5.0f;
constexpr int K2T = K2TV;
constexpr int K2W = K2T / 64;
constexpr int TCAP = (NB <= 32) ? 320 : (NB <= 64 ? 512 : 832);
constexpr int CAPN = (S >= 4) ? 32 : 48;
constexpr u32 OOB = 0x80000000u;
constexpr u32 INV = 0xffffffffu;
constexpr u32 MORE = 0xfffffffeu;

__device__ __forceinline__ float lo2f(u32 x) { return __uint_as_float(x << 16); }
__device__ __forceinline__ float hi2f(u32 x) { return __uint_as_float(x & 0xffff0000u); }
__device__ __forceinline__ u32 pack2(float a, float b) {
    bf2 r = {(__bf16)a, (__bf16)b};
    return __builtin_bit_cast(u32, r);
}
__device__ __forceinline__ void dma16(const void* g, void* l) {
    __builtin_amdgcn_global_load_lds((const u32*)g, (u32*)l, 16, 0, 0);
}
__device__ __forceinline__ void dma16_hidden(const void* g, void* l) {
    unsigned keep;
    const unsigned dstl = __builtin_amdgcn_readfirstlane((unsigned)(size_t)(__attribute__((address_space(3))) char*)l);
    asm volatile("s_mov_b32 %0, m0\n\ts_mov_b32 m0, %2\n\ts_nop 0\n\tglobal_load_lds_dwordx4 %1, off\n\ts_mov_b32 m0, %0"
                 : "=&s"(keep) : "v"(g), "s"(dstl) : "memory");
}
__device__ __forceinline__ s4 tr_read(const char* p) {
    return __builtin_amdgcn_ds_read_tr16_b64_v4i16((lds_s4p)(lds_vp)(p));
}
__device__ __forceinline__ bf8 cat8(s4 a, s4 b) {
    s8 r = __builtin_shufflevector(a, b, 0, 1, 2, 3, 4, 5, 6, 7);
    return __builtin_bit_cast(bf8, r);
}
__device__ __forceinline__ u32 mbcnt64(unsigned long long m) {
    return __builtin_amdgcn_mbcnt_hi((u32)(m >> 32), __builtin_amdgcn_mbcnt_lo((u32)m, 0));
}
__device__ __forceinline__ int wave_incl_scan(int v) {
    v += __builtin_amdgcn_update_dpp(0, v, 0x111, 0xf, 0xf, false);
    v += __builtin_amdgcn_update_dpp(0, v, 0x112, 0xf, 0xf, false);
    v += __builtin_amdgcn_update_dpp(0, v, 0x114, 0xf, 0xf, false);
    v += __builtin_amdgcn_update_dpp(0, v, 0x118, 0xf, 0xf, false);
    v += __builtin_amdgcn_update_dpp(0, v, 0x142, 0xa, 0xf, false);
    v += __builtin_amdgcn_update_dpp(0, v, 0x143, 0xc, 0xf, false);
    return v;
}
__device__ __forceinline__ void lds_barrier() {
    asm volatile("s_waitcnt lgkmcnt(0)" ::: "memory");
    __builtin_amdgcn_s_barrier();
    asm volatile("" ::: "memory");
}

__device__ __forceinline__ void gemm_role(char* lds, int tile, const u16* __restrict__ sv, const u16* __restrict__ w1,
                                          const u16* __restrict__ w2, char* __restrict__ tmain, char* __restrict__ tz,
                                          char* __restrict__ rv, int N) {
    const int tid = threadIdx.x, lane = tid & 63, w = __builtin_amdgcn_readfirstlane(tid >> 6);
    const int n0 = tile * TM;
    const int l4 = lane >> 4, l15 = lane & 15;
#pragma unroll
    for (int i = 0; i < 2; ++i) {
        const int row = (2 * w + i) * 4 + l4, c = l15 ^ (row & 15);
        const int node = min(n0 + row, N - 1);
        dma16((const char*)sv + (size_t)node * 1024 + c * 16, lds + OFF_S + (2 * w + i) * 1024);
    }
    const u32 csw = (((l15 >> 2) ^ l4) << 6) | ((l15 & 3) << 4);
    {
        const char* g1 = (const char*)w1 + l4 * 256 + csw;
#pragma unroll
        for (int i = 0; i < 4; ++i) dma16(g1 + (4 * w + i) * 1024, lds + OFF_W1 + (4 * w + i) * 1024);
        const char* g2 = (const char*)w2 + l4 * 768 + csw;
#pragma unroll
        for (int t = 0; t < 3; ++t)
#pragma unroll
            for (int i = 0; i < 4; ++i) dma16(g2 + (4 * w + i) * 3072 + t * 256, lds + OFF_W2 + t * 32768 + (4 * w + i) * 1024);
    }
    __builtin_amdgcn_sched_barrier(0);
    u32 vreg[8][4];
    if (USE_RV) {
#pragma unroll
        for (int i = 0; i < 8; ++i) {
            const int node = min(n0 + 8 * w + i, N - 1);
            const char* p = (const char*)sv + (size_t)node * 1024;
#pragma unroll
            for (int q = 0; q < 4; ++q) {
                u32 t;
                asm volatile("global_load_dword %0, %1, %2 offset:%3" : "=v"(t) : "v"(lane * 4), "s"(p), "i"(q * 256) : "memory");
                vreg[i][q] = t;
            }
        }
    }
    __builtin_amdgcn_sched_barrier(0);
    if (USE_RV) asm volatile("s_waitcnt vmcnt(44)" ::: "memory"); else asm volatile("s_waitcnt vmcnt(12)" ::: "memory");
    __builtin_amdgcn_s_barrier();
    __builtin_amdgcn_sched_barrier(0);

    const int r = lane & 31, h = lane >> 5, g = lane >> 4, tq = (lane & 15) >> 2, tp = lane & 3;
    const int nt = w & 1;
    const int row = 32 * nt + r;
    const int xsw = (h ^ (r & 15)) << 4;
    const int trl = (8 * (g >> 1) + tq) * 256 + 32 * (g & 1) + 8 * tp;
    {
        const int ht = w >> 1;
        const char* abase = lds + OFF_W1 + trl + ((ht ^ tq) << 6);
        const char* bbase = lds + OFF_S + row * 256;
        f16v acc = {};
#pragma unroll
        for (int ks = 0; ks < 8; ++ks) {
            s4 a0 = tr_read(abase + ks * 4096), a1 = tr_read(abase + ks * 4096 + 1024);
            bf8 b = *(const bf8*)(bbase + ((ks * 32) ^ xsw));
            acc = __builtin_amdgcn_mfma_f32_32x32x16_bf16(cat8(a0, a1), b, acc, 0, 0, 0);
        }
        char* hbase = lds + OFF_H + row * 256 + 8 * h;
#pragma unroll
        for (int gg = 0; gg < 4; ++gg) {
            float y[4];
#pragma unroll
            for (int j = 0; j < 4; ++j) { float a = acc[4 * gg + j]; y[j] = a * __builtin_amdgcn_rcpf(1.0f + __expf(-a)); }
            u2 o = {pack2(y[0], y[1]), pack2(y[2], y[3])};
            *(u2*)(hbase + (((4 * ht + gg) ^ (r & 15)) << 4)) = o;
        }
    }
    asm volatile("s_waitcnt vmcnt(0) lgkmcnt(0)" ::: "memory");
    __builtin_amdgcn_s_barrier();
    __builtin_amdgcn_sched_barrier(0);
#pragma unroll
    for (int i = 0; i < 6; ++i) {
        const int rr = (6 * w + i) * 4 + l4;
        const int nl = rr / 3, q = rr - nl * 3;
        const int node = min(n0 + nl, N - 1);
        dma16((const char*)sv + (size_t)node * 1024 + (q + 1) * 256 + ((l15 ^ (nl & 15)) << 4), lds + OFF_V + (6 * w + i) * 1024);
    }
    if (USE_RV) {
        auto rr = __builtin_amdgcn_make_buffer_rsrc((void*)rv, 0, (u32)N * 1024u, 0x00020000);
        const int sl = lane / G, gl = lane % G;
        const u32 lo = (u32)sl * N * (16 * G) + gl * 4;
#pragma unroll
        for (int i = 0; i < 8; ++i) {
            const int node = n0 + 8 * w + i;
            const u32 lo_ = (node < N) ? lo : OOB;
#pragma unroll
            for (int q = 0; q < 4; ++q) __builtin_amdgcn_raw_buffer_store_b32(vreg[i][q], rr, lo_ + q * (4 * G), min(node, N - 1) * (16 * G), ST_AUX);
        }
    }
    f16v acc2[3] = {};
    const int cb = w >> 1;
    {
        const char* bbase = lds + OFF_H + row * 256;
        const char* ab = lds + OFF_W2 + trl + ((cb ^ tq) << 6);
#pragma unroll
        for (int ks = 0; ks < 8; ++ks) {
            bf8 b = *(const bf8*)(bbase + ((ks * 32) ^ xsw));
#pragma unroll
            for (int t = 0; t < 3; ++t) {
                s4 a0 = tr_read(ab + t * 32768 + ks * 4096), a1 = tr_read(ab + t * 32768 + ks * 4096 + 1024);
                acc2[t] = __builtin_amdgcn_mfma_f32_32x32x16_bf16(cat8(a0, a1), b, acc2[t], 0, 0, 0);
            }
        }
    }
    if (USE_RV) asm volatile("s_waitcnt vmcnt(32) lgkmcnt(0)" ::: "memory"); else asm volatile("s_waitcnt vmcnt(0) lgkmcnt(0)" ::: "memory");
    __builtin_amdgcn_s_barrier();
    __builtin_amdgcn_sched_barrier(0);
    {
        char* rrow = lds + row * RPITCH;
        const char* vrow = lds + OFF_V + row * 768 + 8 * h;
#pragma unroll
        for (int gg = 0; gg < 4; ++gg) {
            const int csw2 = ((4 * cb + gg) ^ (r & 15)) << 4;
            const u2 vx = *(const u2*)(vrow + csw2), vy = *(const u2*)(vrow + 256 + csw2), vz = *(const u2*)(vrow + 512 + csw2);
            const float a0 = acc2[1][4 * gg], a1 = acc2[1][4 * gg + 1], a2 = acc2[1][4 * gg + 2], a3 = acc2[1][4 * gg + 3];
            const int c0 = 32 * cb + 8 * gg + 4 * h;
            u4 o0 = {pack2(acc2[0][4 * gg], acc2[0][4 * gg + 1]), pack2(acc2[2][4 * gg], acc2[2][4 * gg + 1]),
                     pack2(a0 * lo2f(vx[0]), a1 * hi2f(vx[0])), pack2(a0 * lo2f(vy[0]), a1 * hi2f(vy[0]))};
            u4 o1 = {pack2(acc2[0][4 * gg + 2], acc2[0][4 * gg + 3]), pack2(acc2[2][4 * gg + 2], acc2[2][4 * gg + 3]),
                     pack2(a2 * lo2f(vx[1]), a3 * hi2f(vx[1])), pack2(a2 * lo2f(vy[1]), a3 * hi2f(vy[1]))};
            u2 oz = {pack2(a0 * lo2f(vz[0]), a1 * hi2f(vz[0])), pack2(a2 * lo2f(vz[1]), a3 * hi2f(vz[1]))};
            *(u4*)(rrow + c0 * 8) = o0;
            *(u4*)(rrow + c0 * 8 + 16) = o1;
            *(u2*)(rrow + 1024 + c0 * 2) = oz;
        }
    }
    lds_barrier();
    __builtin_amdgcn_sched_barrier(0);
    {
        auto rm = __builtin_amdgcn_make_buffer_rsrc((void*)tmain, 0, (u32)N * 1024u, 0x00020000);
        const int gq = lane / G, gl = lane % G;
#pragma unroll
        for (int i = 0; i < 8; ++i) {
            const int p = w * 8 + i;
            const int sl = p % S, nb = (p / S) * S;
            const int nn = nb + gq;
            u4 v = *(const u4*)(lds + nn * RPITCH + (sl * G + gl) * 16);
            const int node = n0 + nn;
            u32 off = (node < N) ? ((u32)(sl * N + node) * G + gl) * 16u : OOB;
            __builtin_amdgcn_raw_buffer_store_b128(v, rm, off, 0, ST_AUX);
        }
        auto rz = __builtin_amdgcn_make_buffer_rsrc((void*)tz, 0, (u32)N * 256u, 0x00020000);
#pragma unroll
        for (int i = 0; i < 2; ++i) {
            const int q = (w * 2 + i) * 64 + lane;
            constexpr int CPN = G / 4;
            const int sl = q / (64 * CPN), rem = q % (64 * CPN), nn = rem / CPN, ck = rem % CPN;
            u4 v = *(const u4*)(lds + nn * RPITCH + 1024 + (sl * G + ck * 4) * 4);
            const int node = n0 + nn;
            u32 off = (node < N) ? ((u32)(sl * N + node) * G + ck * 4) * 4u : OOB;
            __builtin_amdgcn_raw_buffer_store_b128(v, rz, off, 0, ST_AUX);
        }
    }
}

template <bool POS_LDS>
__device__ __forceinline__ void get_pos(const char* lds, const u16* __restrict__ pos, u32 n, float& x, float& y, float& z) {
    if (POS_LDS) {
        const u32* pp = (const u32*)(lds + ((n * 6u) & ~3u));
        const u32 d0 = pp[0], d1 = pp[1];
        const u32 sh = n << 4;
        const u32 xy = __builtin_amdgcn_alignbit(d1, d0, sh);
        const u32 zz = d1 >> (sh & 31);
        x = lo2f(xy); y = hi2f(xy); z = lo2f(zz);
    } else {
        x = __uint_as_float((u32)pos[3 * n] << 16);
        y = __uint_as_float((u32)pos[3 * n + 1] << 16);
        z = __uint_as_float((u32)pos[3 * n + 2] << 16);
    }
}

template <bool POS_LDS>
__device__ __forceinline__ void filter_role(char* lds, int f, int chunk, const u16* __restrict__ pos, const int* __restrict__ src,
                                            const int* __restrict__ dst, u4* __restrict__ cells, u32* __restrict__ cnts, u4* __restrict__ ovf,
                                            u32* __restrict__ ovfp, u32* __restrict__ ovfn, int N, int E, int P) {
    const int tid = threadIdx.x, lane = tid & 63, w = __builtin_amdgcn_readfirstlane(tid >> 6);
    const int e0 = f * chunk, n = min(E, e0 + chunk) - e0;
    u32* eS = (u32*)(lds + POS_BYTES);
    u32* eD = eS + chunk;
    u16* queue = (u16*)(eD + chunk);
    u32* ocnt = (u32*)(lds + POS_BYTES + chunk * 10);
    u32* hist = ocnt + 4;
    {
        const int n4 = n >> 2;
        for (int q0 = w * 64; q0 < n4; q0 += K1T) {
            const int q = q0 + lane;
            if (q < n4) { dma16(src + e0 + q * 4, (char*)eS + q0 * 16); dma16(dst + e0 + q * 4, (char*)eD + q0 * 16); }
        }
        if (tid < 4) {
            const int e = n4 * 4 + tid;
            if (e < chunk) { eS[e] = e < n ? src[e0 + e] : 0; eD[e] = e < n ? dst[e0 + e] : 0; }
        }
    }
    if (POS_LDS) {
        const int bytes = N * 6, nch = bytes >> 4;
        for (int p = w; p * 64 < nch; p += 8) {
            int q = p * 64 + lane;
            if (q < nch) dma16((const char*)pos + q * 16, lds + p * 1024);
        }
        const int rem = (bytes - (nch << 4)) >> 1;
        if (tid < rem) ((u16*)lds)[nch * 8 + tid] = pos[nch * 8 + tid];
    }
    for (int i = tid; i < P; i += K1T) hist[i] = 0;
    if (tid == 0) { ocnt[0] = 0; ocnt[1] = 0; }
    asm volatile("s_waitcnt vmcnt(0) lgkmcnt(0)" ::: "memory");
    __builtin_amdgcn_s_barrier();
    asm volatile("" ::: "memory");
    u32 actmask = 0;
#pragma unroll
    for (int it = 0; it < 4; ++it) {
        const int e = (it * K1T + tid) * 4;
        if (e < n) {
            const u4 a = *(const u4*)(eS + e), b = *(const u4*)(eD + e);
            u32 m4 = 0;
#pragma unroll
            for (int j = 0; j < 4; ++j) {
                float ax, ay, az, bx, by, bz;
                get_pos<POS_LDS>(lds, pos, a[j], ax, ay, az);
                get_pos<POS_LDS>(lds, pos, b[j], bx, by, bz);
                const float rx = ax - bx, ry = ay - by, rz = az - bz;
                const float d2 = __builtin_fmaf(rz, rz, __builtin_fmaf(ry, ry, __builtin_fmaf(rx, rx, 1e-8f)));
                m4 |= (d2 >= CUTOFF * CUTOFF && d2 <= 3.0e38f) ? 0u : (1u << j);
            }
            if (n - e < 4) m4 &= (1u << (n - e)) - 1u;
            actmask |= m4 << (it * 4);
        }
    }
    {
        const int cntt = __popc(actmask);
        const int inc = wave_incl_scan(cntt);
        u32 qb = 0;
        if (lane == 63) qb = atomicAdd(&ocnt[1], (u32)inc);
        qb = __builtin_amdgcn_readlane(qb, 63) + (inc - cntt);
        u32 m = actmask;
        while (m) {
            const int bit = __builtin_ctz(m); m &= m - 1;
            queue[qb++] = (u16)((((bit >> 2) * K1T + tid) << 2) + (bit & 3));
        }
    }
    lds_barrier();
    const int qn = ocnt[1];
    u4* cl = cells + (size_t)f * P * CAPC;
    for (int j = tid; j < qn; j += K1T) {
        const int idx = queue[j];
        const u32 s = eS[idx], dd = eD[idx];
        const u32 p = dd / NB;
        const u32 rank = atomicAdd(&hist[p], 1u);
        float ax, ay, az, bx, by, bz;
        get_pos<POS_LDS>(lds, pos, s, ax, ay, az);
        get_pos<POS_LDS>(lds, pos, dd, bx, by, bz);
        const u4 r = {s | ((dd - p * NB) << 24), __float_as_uint(ax - bx), __float_as_uint(ay - by), __float_as_uint(az - bz)};
        if (rank < CAPC) cl[p * CAPC + rank] = r;
        else { const u32 o = atomicAdd(&ocnt[0], 1u); ovf[(size_t)f * chunk + o] = r; ovfp[(size_t)f * chunk + o] = p; }
    }
    lds_barrier();
    for (int p = tid; p < P; p += K1T) cnts[(size_t)f * P + p] = hist[p];
    if (tid == 0) ovfn[f] = ocnt[0];
}

__global__ __launch_bounds__(K1T) void k1_kernel(const u16* __restrict__ sv, const u16* __restrict__ pos, const int* __restrict__ src,
                                                 const int* __restrict__ dst, const u16* __restrict__ w1, const u16* __restrict__ w2,
                                                 char* __restrict__ tmain, char* __restrict__ tz, char* __restrict__ rv,
                                                 u4* __restrict__ cells, u32* __restrict__ cnts, u4* __restrict__ ovf, u32* __restrict__ ovfp, u32* __restrict__ ovfn,
                                                 int N, int E, int nfilt, int chunk, int P) {
    __shared__ __attribute__((aligned(1024))) char lds[LDS_BYTES];
    const int b = blockIdx.x;
    if (b < nfilt) {
        if (N * 6 + 16 <= POS_BYTES) filter_role<true>(lds, b, chunk, pos, src, dst, cells, cnts, ovf, ovfp, ovfn, N, E, P);
        else filter_role<false>(lds, b, chunk, pos, src, dst, cells, cnts, ovf, ovfp, ovfn, N, E, P);
    } else {
        gemm_role(lds, b - nfilt, sv, w1, w2, tmain, tz, rv, N);
    }
}

template <int I> __device__ __forceinline__ u32 qb(u32 x) {
    return __builtin_amdgcn_update_dpp(0u, x, I | (I << 2) | (I << 4) | (I << 6), 0xf, 0xf, true);
}
template <int I> __device__ __forceinline__ float qbf(u32 x) { return __uint_as_float(qb<I>(x)); }
__device__ __forceinline__ float fast_sin_rev(float rev) { return __builtin_amdgcn_sinf(__builtin_amdgcn_fractf(rev)); }
__device__ __forceinline__ float fast_cos_rev(float rev) { return __builtin_amdgcn_cosf(__builtin_amdgcn_fractf(rev)); }

struct Buf { u4 d[4]; u32 z[4]; u4 rb; u32 pn; };

__global__ __launch_bounds__(K2T, 4) void k2_kernel(const u16* __restrict__ sv, const u16* __restrict__ freqs, const u16* __restrict__ wr,
                                                    const u4* __restrict__ cells, const u32* __restrict__ cnts, const u4* __restrict__ ovf,
                                                    const u32* __restrict__ ovfp, const u32* __restrict__ ovfn,
                                                    const char* __restrict__ tmain, const char* __restrict__ tz, const char* __restrict__ rv,
                                                    u16* __restrict__ out, int N, int C, int chunk, int P) {
    __shared__ u4 recsA[TCAP + 1];
    __shared__ u4 recsB[TCAP + 1];
    __shared__ u32 keys[NB * CAPN];
    __shared__ u16 Lx[NB * CAPN];
    __shared__ u16 slotL[TCAP];
    __shared__ __attribute__((aligned(16))) int ncnt[(NB + 3) / 4 * 4];
    __shared__ unsigned char order[NB];
    __shared__ int Tsh, tetctr, slow, slow2, novl, ovl[8];
    __shared__ __attribute__((aligned(16))) u32 resL[K2W][2][256];
    const int tid = threadIdx.x, lane = tid & 63, w = __builtin_amdgcn_readfirstlane(tid >> 6);
    constexpr int NP = 8 / S;
    const int x = blockIdx.x & 7, j = blockIdx.x >> 3;
    const int slice = x % S, part = x / S;
    const int p = j * NP + part;
    if (p >= P) return;
    const int n0 = p * NB;
    const int grp = lane / G, gl = lane % G;
    const int ch = slice * (128 / S) + 2 * gl;
    auto rm = __builtin_amdgcn_make_buffer_rsrc((void*)(tmain + (size_t)slice * N * (G * 16)), 0, (u32)N * (G * 16), 0x00020000);
    auto rz = __builtin_amdgcn_make_buffer_rsrc((void*)(tz + (size_t)slice * N * (G * 4)), 0, (u32)N * (G * 4), 0x00020000);
    auto ro = __builtin_amdgcn_make_buffer_rsrc((void*)out, 0, (u32)N * 1024u, 0x00020000);

    constexpr int RPT = (64 * CAPC + K2T - 1) / K2T;
    const int nslots = C * CAPC;
    const u32 cstride = (u32)P * CAPC * 16;
    auto rc = __builtin_amdgcn_make_buffer_rsrc((void*)(cells + (size_t)p * CAPC), 0, (u32)C * cstride - (u32)p * CAPC * 16, 0x00020000);
    auto rn = __builtin_amdgcn_make_buffer_rsrc((void*)(cnts + p), 0, ((u32)C * P - p) * 4, 0x00020000);
    u4 raw[RPT]; u32 rcnt[RPT];
#pragma unroll
    for (int k = 0; k < RPT; ++k) {
        const u32 q = tid + k * K2T, c = q / CAPC;
        raw[k] = __builtin_amdgcn_raw_buffer_load_b128(rc, c * cstride + (q % CAPC) * 16, 0, 0);
        rcnt[k] = __builtin_amdgcn_raw_buffer_load_b32(rn, c * (u32)P * 4, 0, 0);
    }
    const u32 fq = freqs[lane & 3];
    __builtin_amdgcn_sched_barrier(0);
    if (tid < (NB + 3) / 4 * 4) ncnt[tid] = (tid < NB) ? 0 : -1;
    if (tid == 64) { recsA[TCAP] = u4{0, 0, 0, 0}; recsB[TCAP] = u4{OOB, 0, 0, 0}; Tsh = 0; tetctr = K2W; slow2 = 0; slow = (nslots > RPT * K2T) ? 1 : 0; novl = 0; }
    u32 wk[3][4];
#pragma unroll
    for (int t = 0; t < 3; ++t)
#pragma unroll
        for (int k = 0; k < 4; ++k) wk[t][k] = *(const u32*)(wr + k * 384 + t * 128 + ch);
    lds_barrier();
    auto push_raw = [&](const u4& r, bool valid) {
        const unsigned long long m = __ballot(valid);
        u32 ub = 0;
        if (lane == 0) ub = atomicAdd(&Tsh, (int)__popcll(m));
        const u32 u = __builtin_amdgcn_readfirstlane(ub) + mbcnt64(m);
        if (valid && u < TCAP) recsB[u] = r;
    };
#pragma unroll
    for (int k = 0; k < RPT; ++k) {
        const u32 q = tid + k * K2T;
        if (rcnt[k] > CAPC && (q % CAPC) == 0) { const int o = atomicAdd(&novl, 1); if (o < 8) ovl[o] = q / CAPC; else slow = 1; }
        push_raw(raw[k], (q % CAPC) < rcnt[k]);
    }
    lds_barrier();
    if (slow | novl) {
        if (slow) {
            lds_barrier();
            if (tid == 0) Tsh = TCAP + 1;
        } else {
            const int no_ = novl;
            for (int oi = 0; oi < no_; ++oi) {
                const int c = ovl[oi], no = ovfn[c];
                for (int i0 = 0; i0 < no; i0 += K2T) {
                    const int i = min(i0 + tid, no - 1);
                    const u32 pp = ovfp[(size_t)c * chunk + i];
                    const u4 r = ovf[(size_t)c * chunk + i];
                    push_raw(r, i0 + tid < no && pp == (u32)p);
                }
            }
        }
        lds_barrier();
    }
    float fr[4];
#pragma unroll
    for (int k = 0; k < 4; ++k) fr[k] = __uint_as_float(__builtin_amdgcn_readlane(fq, k) << 16) * 0.15915494309189535f;
    const int T = Tsh;
    for (int u = tid; u < min(T, TCAP); u += K2T) {
        const u4 r = recsB[u];
        const int dl = r[0] >> 24;
        const int sl = atomicAdd(&ncnt[dl], 1);
        const u32 key = (r[0] & 0xffffffu) * (G * 16);
        if (sl < CAPN) keys[dl * CAPN + sl] = key; else slow2 = 1;
        slotL[u] = (u16)((dl << 8) | (sl & 255));
        const float rx = __uint_as_float(r[1]), ry = __uint_as_float(r[2]), rz_ = __uint_as_float(r[3]);
        const float d = __builtin_amdgcn_sqrtf(__builtin_fmaf(rz_, rz_, __builtin_fmaf(ry, ry, __builtin_fmaf(rx, rx, 1e-8f))));
        const float inv = __builtin_amdgcn_rcpf(d);
        const float fc = (d < CUTOFF) ? 0.5f * fast_cos_rev(d * (0.5f / CUTOFF)) + 0.5f : 0.0f * d;
        const float sc = inv * fc;
        float gk[4];
#pragma unroll
        for (int q = 0; q < 4; ++q) gk[q] = fast_sin_rev(d * fr[q]) * sc;
        const u32 h01 = pack2(gk[0], gk[1]), h23 = pack2(gk[2], gk[3]);
        const u32 l01 = pack2(gk[0] - lo2f(h01), gk[1] - hi2f(h01)), l23 = pack2(gk[2] - lo2f(h23), gk[3] - hi2f(h23));
        recsA[u] = u4{h01, h23, l01, l23};
        recsB[u] = u4{key, __float_as_uint(rx * inv), __float_as_uint(ry * inv), __float_as_uint(rz_ * inv)};
    }
    s4 wb[3][2];
#pragma unroll
    for (int t = 0; t < 3; ++t) {
        u2 b0 = {__builtin_amdgcn_perm(wk[t][1], wk[t][0], 0x05040100), __builtin_amdgcn_perm(wk[t][3], wk[t][2], 0x05040100)};
        u2 b1 = {__builtin_amdgcn_perm(wk[t][1], wk[t][0], 0x07060302), __builtin_amdgcn_perm(wk[t][3], wk[t][2], 0x07060302)};
        wb[t][0] = __builtin_bit_cast(s4, b0);
        wb[t][1] = __builtin_bit_cast(s4, b1);
    }
    lds_barrier();
    if (T > TCAP || slow2) {
        float frs[4];
#pragma unroll
        for (int k = 0; k < 4; ++k) frs[k] = __uint_as_float((u32)freqs[k] << 16);
        for (int tet = w; tet < NB / S; tet += K2W) {
            const int nl = tet * S + grp, node = n0 + nl;
            float acc[4][2] = {};
            for (int c = 0; c < 2 * C; ++c) {
                const int cc = c < C ? c : c - C;
                const u32 cn = cnts[(size_t)cc * P + p];
                const int l = c < C ? (int)min(cn, (u32)CAPC) : (cn > CAPC ? (int)ovfn[cc] : 0);
                for (int base = 0; base < l; base += 64) {
                    u4 rr = {INV, 0, 0, 0};
                    bool ok = base + lane < l;
                    if (ok) {
                        if (c < C) rr = cells[((size_t)cc * P + p) * CAPC + base + lane];
                        else if (ovfp[(size_t)cc * chunk + base + lane] == (u32)p) rr = ovf[(size_t)cc * chunk + base + lane];
                        else ok = false;
                    }
                    const int dl = rr[0] >> 24;
                    unsigned long long m = __ballot(ok && (dl / S == tet));
                    while (m) {
                        const int i = __builtin_ctzll(m); m &= m - 1;
                        const u32 q0 = __builtin_amdgcn_readlane(rr[0], i);
                        const float rx = __uint_as_float(__builtin_amdgcn_readlane(rr[1], i)), ry = __uint_as_float(__builtin_amdgcn_readlane(rr[2], i)),
                                    rz_ = __uint_as_float(__builtin_amdgcn_readlane(rr[3], i));
                        const float d = sqrtf(rx * rx + ry * ry + rz_ * rz_ + 1e-8f), inv = 1.0f / d;
                        const float fc = (d < CUTOFF) ? 0.5f * (cosf(3.14159265358979323846f * d / CUTOFF) + 1.0f) : 0.0f * d;
                        float W[3][2] = {};
#pragma unroll 1
                        for (int k = 0; k < 4; ++k) {
                            const float fk = k == 0 ? frs[0] : (k == 1 ? frs[1] : (k == 2 ? frs[2] : frs[3]));
                            const float gk = sinf(d * fk) * inv * fc;
#pragma unroll
                            for (int t = 0; t < 3; ++t) {
                                const u32 wv = k == 0 ? wk[t][0] : (k == 1 ? wk[t][1] : (k == 2 ? wk[t][2] : wk[t][3]));
                                W[t][0] += gk * lo2f(wv); W[t][1] += gk * hi2f(wv);
                            }
                        }
                        if ((int)(q0 >> 24) == nl) {
                            const u32 off = ((q0 & 0xffffffu) * G + gl) * 4u;
                            const u4 dd = __builtin_amdgcn_raw_buffer_load_b128(rm, off * 4u, 0, 0);
                            const u32 zz = __builtin_amdgcn_raw_buffer_load_b32(rz, off, 0, 0);
                            acc[0][0] += W[0][0] * lo2f(dd[0]); acc[0][1] += W[0][1] * hi2f(dd[0]);
                            const float t0 = W[2][0] * lo2f(dd[1]), t1 = W[2][1] * hi2f(dd[1]);
                            acc[1][0] += W[1][0] * lo2f(dd[2]) + t0 * rx * inv; acc[1][1] += W[1][1] * hi2f(dd[2]) + t1 * rx * inv;
                            acc[2][0] += W[1][0] * lo2f(dd[3]) + t0 * ry * inv; acc[2][1] += W[1][1] * hi2f(dd[3]) + t1 * ry * inv;
                            acc[3][0] += W[1][0] * lo2f(zz) + t0 * rz_ * inv;   acc[3][1] += W[1][1] * hi2f(zz) + t1 * rz_ * inv;
                        }
                    }
                }
            }
            if (node < N) {
#pragma unroll
                for (int q = 0; q < 4; ++q) {
                    const u32 r = *(const u32*)((const char*)sv + (size_t)node * 1024 + q * 256 + ch * 2);
                    *(u32*)((char*)out + (size_t)node * 1024 + q * 256 + ch * 2) = pack2(lo2f(r) + acc[q][0], hi2f(r) + acc[q][1]);
                }
            }
        }
        return;
    }
    if (tid >= K2T - 128 && tid - (K2T - 128) < NB) {
        const int n = tid - (K2T - 128), c = ncnt[n];
        int rk = 0;
        for (int i = 0; i < NB; i += 4) {
            typedef int i4 __attribute__((ext_vector_type(4)));
            const i4 q = *(const i4*)(ncnt + i);
#pragma unroll
            for (int k = 0; k < 4; ++k) rk += (q[k] > c || (q[k] == c && i + k < n)) ? 1 : 0;
        }
        order[rk] = (unsigned char)n;
    }
    for (int u = tid; u < T; u += K2T) {
        const int ds = slotL[u], dl = ds >> 8, sl = ds & 255;
        const int c = ncnt[dl];
        const u32 key = recsB[u][0];
        int rank = 0;
        for (int i = 0; i < c; ++i) { u32 ki = keys[dl * CAPN + i]; rank += (ki < key || (ki == key && i < sl)) ? 1 : 0; }
        Lx[dl * CAPN + rank] = (u16)u;
    }
    lds_barrier();
    float as0 = 0, as1 = 0, ax0 = 0, ax1 = 0, ay0 = 0, ay1 = 0, az0 = 0, az1 = 0;
    const u32 gl16 = gl * 16;
    int i_tet = -1, i_b = 0, i_mm = 0, i_st = 0, i_c = 0, i_nl = 0, i_par = 0;
    auto advance = [&]() -> bool {
        i_b += 4;
        if (i_b < i_mm) return true;
        if (i_tet < 0) i_tet = w;
        else {
            int t = 0;
            if (lane == 0) t = atomicAdd(&tetctr, 1);
            i_tet = __builtin_amdgcn_readfirstlane(t);
        }
        if (i_tet >= NB / S) return false;
        const int nl = order[i_tet * S + grp];
        i_nl = nl; i_st = nl * CAPN; i_c = ncnt[nl];
        int mm = 1;
#pragma unroll
        for (int g2 = 0; g2 < S; ++g2) mm = max(mm, __builtin_amdgcn_readlane(i_c, g2 * G));
        i_mm = mm;
        i_b = 0;
        i_par ^= 1;
        {
            constexpr int CPR = G / 4;
            const int rem = lane % (4 * CPR);
            const int node = min(n0 + (int)order[i_tet * S + lane / (4 * CPR)], N - 1);
            const char* src_ = USE_RV ? rv + ((size_t)slice * N + node) * (16 * G) + rem * 16
                                      : (const char*)sv + (size_t)node * 1024 + (rem / CPR) * 256 + slice * (G * 4) + (rem % CPR) * 16;
            dma16_hidden(src_, (char*)resL[w][i_par]);
        }
        return true;
    };
#define ISSUE1(B, I) { \
        const u32 off_ = qb<I>(B.rb[0]) + gl16; \
        B.d[I] = __builtin_amdgcn_raw_buffer_load_b128(rm, off_, 0, 0); \
        B.z[I] = __builtin_amdgcn_raw_buffer_load_b32(rz, off_ >> 2, 0, 0); }
#define ISSUE(B, OK) { \
        const int k_ = i_b + (lane & 3); \
        int idx_ = Lx[min(i_st + k_, NB * CAPN - 1)]; \
        idx_ = ((OK) && k_ < i_c) ? idx_ : TCAP; \
        B.pn = (u32)idx_ | ((u32)i_nl << 16); \
        B.rb = recsB[idx_]; \
        ISSUE1(B, 0) ISSUE1(B, 1) ISSUE1(B, 2) ISSUE1(B, 3) \
        C_##B##_n = (OK) ? min(4, i_mm - i_b) : 0; C_##B##_par = i_par; }
#define OPQ(X) asm volatile("" : "+v"(X))
#define STEP(B, I) { \
        const float rx = qbf<I>(B.rb[1]), ry = qbf<I>(B.rb[2]), rz_ = qbf<I>(B.rb[3]); \
        const float t0 = Wr0[I] * lo2f(B.d[I][1]), t1 = Wr1[I] * hi2f(B.d[I][1]); \
        as0 = __builtin_fmaf(Ws0[I], lo2f(B.d[I][0]), as0); OPQ(as0); as1 = __builtin_fmaf(Ws1[I], hi2f(B.d[I][0]), as1); OPQ(as1); \
        ax0 = __builtin_fmaf(Wv0[I], lo2f(B.d[I][2]), ax0); OPQ(ax0); ax1 = __builtin_fmaf(Wv1[I], hi2f(B.d[I][2]), ax1); OPQ(ax1); \
        ay0 = __builtin_fmaf(Wv0[I], lo2f(B.d[I][3]), ay0); OPQ(ay0); ay1 = __builtin_fmaf(Wv1[I], hi2f(B.d[I][3]), ay1); OPQ(ay1); \
        az0 = __builtin_fmaf(Wv0[I], lo2f(B.z[I]), az0); OPQ(az0);    az1 = __builtin_fmaf(Wv1[I], hi2f(B.z[I]), az1); OPQ(az1); \
        ax0 = __builtin_fmaf(t0, rx, ax0); OPQ(ax0); ax1 = __builtin_fmaf(t1, rx, ax1); OPQ(ax1); \
        ay0 = __builtin_fmaf(t0, ry, ay0); OPQ(ay0); ay1 = __builtin_fmaf(t1, ry, ay1); OPQ(ay1); \
        az0 = __builtin_fmaf(t0, rz_, az0); OPQ(az0); az1 = __builtin_fmaf(t1, rz_, az1); OPQ(az1); }
#define WC(T, C2) __builtin_amdgcn_mfma_f32_4x4x4bf16_1k(ghi, wb[T][C2], __builtin_amdgcn_mfma_f32_4x4x4bf16_1k(glo, wb[T][C2], zf_, 0, 0, 0), 0, 0, 0)
#define COMPUTE(B) { \
        const f4 zf_ = {0, 0, 0, 0}; \
        const u4 ga_ = recsA[B.pn & 0xffffu]; \
        const u2 ghi_ = {ga_[0], ga_[1]}, glo_ = {ga_[2], ga_[3]}; \
        const s4 ghi = __builtin_bit_cast(s4, ghi_), glo = __builtin_bit_cast(s4, glo_); \
        const f4 Ws0 = WC(0, 0), Ws1 = WC(0, 1), Wv0 = WC(1, 0), Wv1 = WC(1, 1), Wr0 = WC(2, 0), Wr1 = WC(2, 1); \
        asm volatile("" :: "v"(B.d[0]), "v"(B.d[1]), "v"(B.d[2]), "v"(B.d[3]), "v"(B.z[0]), "v"(B.z[1]), "v"(B.z[2]), "v"(B.z[3])); \
        STEP(B, 0) \
        if (C_##B##_n > 1) STEP(B, 1) \
        if (C_##B##_n > 2) STEP(B, 2) \
        if (C_##B##_n > 3) STEP(B, 3) \
        if (C_##B##_fin) { \
            const int nl_ = B.pn >> 16, node_ = n0 + nl_; \
            { \
                const u32* rp = resL[w][C_##B##_par] + grp * (4 * G) + gl; \
                const u32 r0 = rp[0], r1 = rp[G], r2 = rp[2 * G], r3 = rp[3 * G]; \
                const u32 o = (node_ < N) ? (u32)node_ * 1024u + ch * 2 : OOB; \
                __builtin_amdgcn_raw_buffer_store_b32(pack2(lo2f(r0) + as0, hi2f(r0) + as1), ro, o, 0, OUT_AUX); \
                __builtin_amdgcn_raw_buffer_store_b32(pack2(lo2f(r1) + ax0, hi2f(r1) + ax1), ro, o, 256, OUT_AUX); \
                __builtin_amdgcn_raw_buffer_store_b32(pack2(lo2f(r2) + ay0, hi2f(r2) + ay1), ro, o, 512, OUT_AUX); \
                __builtin_amdgcn_raw_buffer_store_b32(pack2(lo2f(r3) + az0, hi2f(r3) + az1), ro, o, 768, OUT_AUX); \
            } \
            as0 = as1 = ax0 = ax1 = ay0 = ay1 = az0 = az1 = 0.f; \
        } }
    {
        Buf A, B;
        int C_A_n, C_B_n, C_A_par, C_B_par; bool C_A_fin, C_B_fin;
        (void)advance();
        ISSUE(A, true) C_A_fin = (i_b + 4 >= i_mm);
        bool m2;
        do {
            const bool m1 = advance();
            ISSUE(B, m1) C_B_fin = m1 && (i_b + 4 >= i_mm);
            COMPUTE(A)
            m2 = m1 && advance();
            ISSUE(A, m2) C_A_fin = m2 && (i_b + 4 >= i_mm);
            COMPUTE(B)
        } while (m2);
    }
}

extern "C" void kernel_launch(void* const* d_in, const int* in_sizes, int n_in, void* d_out, int out_size, void* d_ws, size_t ws_size,
                              hipStream_t stream) {
    const u16* sv = (const u16*)d_in[0];
    const u16* pos = (const u16*)d_in[1];
    const int* src = (const int*)d_in[2];
    const int* dst = (const int*)d_in[3];
    const u16* freqs = (const u16*)d_in[4];
    const u16* w1 = (const u16*)d_in[5];
    const u16* w2 = (const u16*)d_in[6];
    const u16* wr = (const u16*)d_in[7];
    const int N = in_sizes[0] / 512, E = in_sizes[2];
    const int P = (N + NB - 1) / NB;
    const int chmax = std::min(CHMAX, ((LDS_BYTES - POS_BYTES - 4 * P - 64) / 10) & ~3);
    int nfilt = NFILT_MIN;
    while ((((E + nfilt - 1) / nfilt + 3) & ~3) > chmax) ++nfilt;
    const int chunk = ((E + nfilt - 1) / nfilt + 3) & ~3;
    char* ws = (char*)d_ws;
    size_t off = 0;
    auto take = [&](size_t bytes) { char* p = ws + off; off += (bytes + 255) / 256 * 256; return p; };
    char* tmain = take((size_t)N * 1024);
    char* tz = take((size_t)N * 256);
    char* rv = take((size_t)N * 1024);
    u4* cells = (u4*)take((size_t)nfilt * P * CAPC * 16);
    u32* cnts = (u32*)take((size_t)nfilt * P * 4);
    u4* ovf = (u4*)take((size_t)nfilt * chunk * 16);
    u32* ovfp = (u32*)take((size_t)nfilt * chunk * 4);
    u32* ovfn = (u32*)take((size_t)nfilt * 4);
    const int ntiles = (N + TM - 1) / TM;
    k1_kernel<<<nfilt + ntiles, K1T, 0, stream>>>(sv, pos, src, dst, w1, w2, tmain, tz, rv, cells, cnts, ovf, ovfp, ovfn, N, E, nfilt, chunk, P);
    constexpr int NP = 8 / S;
    const int nj = (P + NP - 1) / NP;
    k2_kernel<<<8 * nj, K2T, 0, stream>>>(sv, freqs, wr, cells, cnts, ovf, ovfp, ovfn, tmain, tz, rv, (u16*)d_out, N, nfilt, chunk, P);
}
```

```cpp
#include <hip/hip_runtime.h>
#include <stdint.h>
#include <algorithm>

#define NSLICE 2
#define ST_AUX 16
#define OUT_AUX 16
#define CAPC_ 12
#define DETERMINISTIC 1

typedef unsigned short u16;
typedef unsigned int u32;
typedef short s4 __attribute__((ext_vector_type(4)));
typedef short s8 __attribute__((ext_vector_type(8)));
typedef __bf16 bf2 __attribute__((ext_vector_type(2)));
typedef __bf16 bf8 __attribute__((ext_vector_type(8)));
typedef float f2 __attribute__((ext_vector_type(2)));
typedef float f4 __attribute__((ext_vector_type(4)));
typedef float f16v __attribute__((ext_vector_type(16)));
typedef u32 u2 __attribute__((ext_vector_type(2)));
typedef u32 u4 __attribute__((ext_vector_type(4)));
typedef __attribute__((address_space(3))) s4* lds_s4p;
typedef __attribute__((address_space(3))) void* lds_vp;

constexpr int S = NSLICE;
constexpr int G = 64 / S;
#define NBV 98
#define K2TV 1024
constexpr int NB = NBV;
constexpr int CAPC = (NB <= 32) ? 16 : 32;
#define RVMIN 4
constexpr bool USE_RV = (S >= RVMIN);
constexpr int TM = 64;
constexpr int K1T = 512;
constexpr int NFILT_MIN = 60;
constexpr int EPT = 16;
constexpr int CHMAX = K1T * EPT;
constexpr int LDS_BYTES = 163840;
constexpr int OFF_W2 = 0, OFF_W1 = 98304, OFF_S = 131072, OFF_H = 147456, OFF_V = 98304;
constexpr int RPITCH = 1296;
constexpr int POS_BYTES = 81920;
constexpr float CUTOFF = 5.0f;
constexpr int K2T = K2TV;
constexpr int K2W = K2T / 64;
constexpr int TCAP = (NB <= 32) ? 320 : (NB <= 64 ? 512 : 832);
constexpr int CAPN = (S >= 4) ? 32 : 48;
constexpr u32 OOB = 0x80000000u;
constexpr u32 INV = 0xffffffffu;
constexpr u32 MORE = 0xfffffffeu;

__device__ __forceinline__ float lo2f(u32 x) { return __uint_as_float(x << 16); }
__device__ __forceinline__ float hi2f(u32 x) { return __uint_as_float(x & 0xffff0000u); }
__device__ __forceinline__ u32 pack2(float a, float b) {
    bf2 r = {(__bf16)a, (__bf16)b};
    return __builtin_bit_cast(u32, r);
}
__device__ __forceinline__ void dma16(const void* g, void* l) {
    __builtin_amdgcn_global_load_lds((const u32*)g, (u32*)l, 16, 0, 0);
}
__device__ __forceinline__ void dma16_hidden(const void* g, void* l) {
    unsigned keep;
    const unsigned dstl = __builtin_amdgcn_readfirstlane((unsigned)(size_t)(__attribute__((address_space(3))) char*)l);
    asm volatile("s_mov_b32 %0, m0\n\ts_mov_b32 m0, %2\n\ts_nop 0\n\tglobal_load_lds_dwordx4 %1, off\n\ts_mov_b32 m0, %0"
                 : "=&s"(keep) : "v"(g), "s"(dstl) : "memory");
}
__device__ __forceinline__ s4 tr_read(const char* p) {
    return __builtin_amdgcn_ds_read_tr16_b64_v4i16((lds_s4p)(lds_vp)(p));
}
__device__ __forceinline__ bf8 cat8(s4 a, s4 b) {
    s8 r = __builtin_shufflevector(a, b, 0, 1, 2, 3, 4, 5, 6, 7);
    return __builtin_bit_cast(bf8, r);
}
__device__ __forceinline__ u32 mbcnt64(unsigned long long m) {
    return __builtin_amdgcn_mbcnt_hi((u32)(m >> 32), __builtin_amdgcn_mbcnt_lo((u32)m, 0));
}
__device__ __forceinline__ int wave_incl_scan(int v) {
    v += __builtin_amdgcn_update_dpp(0, v, 0x111, 0xf, 0xf, false);
    v += __builtin_amdgcn_update_dpp(0, v, 0x112, 0xf, 0xf, false);
    v += __builtin_amdgcn_update_dpp(0, v, 0x114, 0xf, 0xf, false);
    v += __builtin_amdgcn_update_dpp(0, v, 0x118, 0xf, 0xf, false);
    v += __builtin_amdgcn_update_dpp(0, v, 0x142, 0xa, 0xf, false);
    v += __builtin_amdgcn_update_dpp(0, v, 0x143, 0xc, 0xf, false);
    return v;
}
__device__ __forceinline__ void lds_barrier() {
    asm volatile("s_waitcnt lgkmcnt(0)" ::: "memory");
    __builtin_amdgcn_s_barrier();
    asm volatile("" ::: "memory");
}

__device__ __forceinline__ void gemm_role(char* lds, int tile, const u16* __restrict__ sv, const u16* __restrict__ w1,
                                          const u16* __restrict__ w2, char* __restrict__ tmain, char* __restrict__ tz,
                                          char* __restrict__ rv, int N) {
    const int tid = threadIdx.x, lane = tid & 63, w = __builtin_amdgcn_readfirstlane(tid >> 6);
    const int n0 = tile * TM;
    const int l4 = lane >> 4, l15 = lane & 15;
#pragma unroll
    for (int i = 0; i < 2; ++i) {
        const int row = (2 * w + i) * 4 + l4, c = l15 ^ (row & 15);
        const int node = min(n0 + row, N - 1);
        dma16((const char*)sv + (size_t)node * 1024 + c * 16, lds + OFF_S + (2 * w + i) * 1024);
    }
    const u32 csw = (((l15 >> 2) ^ l4) << 6) | ((l15 & 3) << 4);
    {
        const char* g1 = (const char*)w1 + l4 * 256 + csw;
#pragma unroll
        for (int i = 0; i < 4; ++i) dma16(g1 + (4 * w + i) * 1024, lds + OFF_W1 + (4 * w + i) * 1024);
        const char* g2 = (const char*)w2 + l4 * 768 + csw;
#pragma unroll
        for (int t = 0; t < 3; ++t)
#pragma unroll
            for (int i = 0; i < 4; ++i) dma16(g2 + (4 * w + i) * 3072 + t * 256, lds + OFF_W2 + t * 32768 + (4 * w + i) * 1024);
    }
    __builtin_amdgcn_sched_barrier(0);
    u32 vreg[8][4];
    if (USE_RV) {
#pragma unroll
        for (int i = 0; i < 8; ++i) {
            const int node = min(n0 + 8 * w + i, N - 1);
            const char* p = (const char*)sv + (size_t)node * 1024;
#pragma unroll
            for (int q = 0; q < 4; ++q) {
                u32 t;
                asm volatile("global_load_dword %0, %1, %2 offset:%3" : "=v"(t) : "v"(lane * 4), "s"(p), "i"(q * 256) : "memory");
                vreg[i][q] = t;
            }
        }
    }
    __builtin_amdgcn_sched_barrier(0);
    if (USE_RV) asm volatile("s_waitcnt vmcnt(44)" ::: "memory"); else asm volatile("s_waitcnt vmcnt(12)" ::: "memory");
    __builtin_amdgcn_s_barrier();
    __builtin_amdgcn_sched_barrier(0);

    const int r = lane & 31, h = lane >> 5, g = lane >> 4, tq = (lane & 15) >> 2, tp = lane & 3;
    const int nt = w & 1;
    const int row = 32 * nt + r;
    const int xsw = (h ^ (r & 15)) << 4;
    const int trl = (8 * (g >> 1) + tq) * 256 + 32 * (g & 1) + 8 * tp;
    {
        const int ht = w >> 1;
        const char* abase = lds + OFF_W1 + trl + ((ht ^ tq) << 6);
        const char* bbase = lds + OFF_S + row * 256;
        f16v acc = {};
#pragma unroll
        for (int ks = 0; ks < 8; ++ks) {
            s4 a0 = tr_read(abase + ks * 4096), a1 = tr_read(abase + ks * 4096 + 1024);
            bf8 b = *(const bf8*)(bbase + ((ks * 32) ^ xsw));
            acc = __builtin_amdgcn_mfma_f32_32x32x16_bf16(cat8(a0, a1), b, acc, 0, 0, 0);
        }
        char* hbase = lds + OFF_H + row * 256 + 8 * h;
#pragma unroll
        for (int gg = 0; gg < 4; ++gg) {
            float y[4];
#pragma unroll
            for (int j = 0; j < 4; ++j) { float a = acc[4 * gg + j]; y[j] = a * __builtin_amdgcn_rcpf(1.0f + __expf(-a)); }
            u2 o = {pack2(y[0], y[1]), pack2(y[2], y[3])};
            *(u2*)(hbase + (((4 * ht + gg) ^ (r & 15)) << 4)) = o;
        }
    }
    asm volatile("s_waitcnt vmcnt(0) lgkmcnt(0)" ::: "memory");
    __builtin_amdgcn_s_barrier();
    __builtin_amdgcn_sched_barrier(0);
#pragma unroll
    for (int i = 0; i < 6; ++i) {
        const int rr = (6 * w + i) * 4 + l4;
        const int nl = rr / 3, q = rr - nl * 3;
        const int node = min(n0 + nl, N - 1);
        dma16((const char*)sv + (size_t)node * 1024 + (q + 1) * 256 + ((l15 ^ (nl & 15)) << 4), lds + OFF_V + (6 * w + i) * 1024);
    }
    if (USE_RV) {
        auto rr = __builtin_amdgcn_make_buffer_rsrc((void*)rv, 0, (u32)N * 1024u, 0x00020000);
        const int sl = lane / G, gl = lane % G;
        const u32 lo = (u32)sl * N * (16 * G) + gl * 4;
#pragma unroll
        for (int i = 0; i < 8; ++i) {
            const int node = n0 + 8 * w + i;
            const u32 lo_ = (node < N) ? lo : OOB;
#pragma unroll
            for (int q = 0; q < 4; ++q) __builtin_amdgcn_raw_buffer_store_b32(vreg[i][q], rr, lo_ + q * (4 * G), min(node, N - 1) * (16 * G), ST_AUX);
        }
    }
    f16v acc2[3] = {};
    const int cb = w >> 1;
    {
        const char* bbase = lds + OFF_H + row * 256;
        const char* ab = lds + OFF_W2 + trl + ((cb ^ tq) << 6);
#pragma unroll
        for (int ks = 0; ks < 8; ++ks) {
            bf8 b = *(const bf8*)(bbase + ((ks * 32) ^ xsw));
#pragma unroll
            for (int t = 0; t < 3; ++t) {
                s4 a0 = tr_read(ab + t * 32768 + ks * 4096), a1 = tr_read(ab + t * 32768 + ks * 4096 + 1024);
                acc2[t] = __builtin_amdgcn_mfma_f32_32x32x16_bf16(cat8(a0, a1), b, acc2[t], 0, 0, 0);
            }
        }
    }
    if (USE_RV) asm volatile("s_waitcnt vmcnt(32) lgkmcnt(0)" ::: "memory"); else asm volatile("s_waitcnt vmcnt(0) lgkmcnt(0)" ::: "memory");
    __builtin_amdgcn_s_barrier();
    __builtin_amdgcn_sched_barrier(0);
    {
        char* rrow = lds + row * RPITCH;
        const char* vrow = lds + OFF_V + row * 768 + 8 * h;
#pragma unroll
        for (int gg = 0; gg < 4; ++gg) {
            const int csw2 = ((4 * cb + gg) ^ (r & 15)) << 4;
            const u2 vx = *(const u2*)(vrow + csw2), vy = *(const u2*)(vrow + 256 + csw2), vz = *(const u2*)(vrow + 512 + csw2);
            const float a0 = acc2[1][4 * gg], a1 = acc2[1][4 * gg + 1], a2 = acc2[1][4 * gg + 2], a3 = acc2[1][4 * gg + 3];
            const int c0 = 32 * cb + 8 * gg + 4 * h;
            u4 o0 = {pack2(acc2[0][4 * gg], acc2[0][4 * gg + 1]), pack2(acc2[2][4 * gg], acc2[2][4 * gg + 1]),
                     pack2(a0 * lo2f(vx[0]), a1 * hi2f(vx[0])), pack2(a0 * lo2f(vy[0]), a1 * hi2f(vy[0]))};
            u4 o1 = {pack2(acc2[0][4 * gg + 2], acc2[0][4 * gg + 3]), pack2(acc2[2][4 * gg + 2], acc2[2][4 * gg + 3]),
                     pack2(a2 * lo2f(vx[1]), a3 * hi2f(vx[1])), pack2(a2 * lo2f(vy[1]), a3 * hi2f(vy[1]))};
            u2 oz = {pack2(a0 * lo2f(vz[0]), a1 * hi2f(vz[0])), pack2(a2 * lo2f(vz[1]), a3 * hi2f(vz[1]))};
            *(u4*)(rrow + c0 * 8) = o0;
            *(u4*)(rrow + c0 * 8 + 16) = o1;
            *(u2*)(rrow + 1024 + c0 * 2) = oz;
        }
    }
    lds_barrier();
    __builtin_amdgcn_sched_barrier(0);
    {
        auto rm = __builtin_amdgcn_make_buffer_rsrc((void*)tmain, 0, (u32)N * 1024u, 0x00020000);
        const int gq = lane / G, gl = lane % G;
#pragma unroll
        for (int i = 0; i < 8; ++i) {
            const int p = w * 8 + i;
            const int sl = p % S, nb = (p / S) * S;
            const int nn = nb + gq;
            u4 v = *(const u4*)(lds + nn * RPITCH + (sl * G + gl) * 16);
            const int node = n0 + nn;
            u32 off = (node < N) ? ((u32)(sl * N + node) * G + gl) * 16u : OOB;
            __builtin_amdgcn_raw_buffer_store_b128(v, rm, off, 0, ST_AUX);
        }
        auto rz = __builtin_amdgcn_make_buffer_rsrc((void*)tz, 0, (u32)N * 256u, 0x00020000);
#pragma unroll
        for (int i = 0; i < 2; ++i) {
            const int q = (w * 2 + i) * 64 + lane;
            constexpr int CPN = G / 4;
            const int sl = q / (64 * CPN), rem = q % (64 * CPN), nn = rem / CPN, ck = rem % CPN;
            u4 v = *(const u4*)(lds + nn * RPITCH + 1024 + (sl * G + ck * 4) * 4);
            const int node = n0 + nn;
            u32 off = (node < N) ? ((u32)(sl * N + node) * G + ck * 4) * 4u : OOB;
            __builtin_amdgcn_raw_buffer_store_b128(v, rz, off, 0, ST_AUX);
        }
    }
}

template <bool POS_LDS>
__device__ __forceinline__ void get_pos(const char* lds, const u16* __restrict__ pos, u32 n, float& x, float& y, float& z) {
    if (POS_LDS) {
        const u32* pp = (const u32*)(lds + ((n * 6u) & ~3u));
        const u32 d0 = pp[0], d1 = pp[1];
        const u32 sh = n << 4;
        const u32 xy = __builtin_amdgcn_alignbit(d1, d0, sh);
        const u32 zz = d1 >> (sh & 31);
        x = lo2f(xy); y = hi2f(xy); z = lo2f(zz);
    } else {
        x = __uint_as_float((u32)pos[3 * n] << 16);
        y = __uint_as_float((u32)pos[3 * n + 1] << 16);
        z = __uint_as_float((u32)pos[3 * n + 2] << 16);
    }
}

template <bool POS_LDS>
__device__ __forceinline__ void filter_role(char* lds, int f, int chunk, const u16* __restrict__ pos, const int* __restrict__ src,
                                            const int* __restrict__ dst, u4* __restrict__ cells, u32* __restrict__ cnts, u4* __restrict__ ovf,
                                            u32* __restrict__ ovfp, u32* __restrict__ ovfn, int N, int E, int P) {
    const int tid = threadIdx.x, lane = tid & 63, w = __builtin_amdgcn_readfirstlane(tid >> 6);
    const int e0 = f * chunk, n = min(E, e0 + chunk) - e0;
    u32* eS = (u32*)(lds + POS_BYTES);
    u32* eD = eS + chunk;
    u16* queue = (u16*)(eD + chunk);
    u32* ocnt = (u32*)(lds + POS_BYTES + chunk * 10);
    u32* hist = ocnt + 4;
    {
        const int n4 = n >> 2;
        for (int q0 = w * 64; q0 < n4; q0 += K1T) {
            const int q = q0 + lane;
            if (q < n4) { dma16(src + e0 + q * 4, (char*)eS + q0 * 16); dma16(dst + e0 + q * 4, (char*)eD + q0 * 16); }
        }
        if (tid < 4) {
            const int e = n4 * 4 + tid;
            if (e < chunk) { eS[e] = e < n ? src[e0 + e] : 0; eD[e] = e < n ? dst[e0 + e] : 0; }
        }
    }
    if (POS_LDS) {
        const int bytes = N * 6, nch = bytes >> 4;
        for (int p = w; p * 64 < nch; p += 8) {
            int q = p * 64 + lane;
            if (q < nch) dma16((const char*)pos + q * 16, lds + p * 1024);
        }
        const int rem = (bytes - (nch << 4)) >> 1;
        if (tid < rem) ((u16*)lds)[nch * 8 + tid] = pos[nch * 8 + tid];
    }
    for (int i = tid; i < P; i += K1T) hist[i] = 0;
    if (tid == 0) { ocnt[0] = 0; ocnt[1] = 0; }
    asm volatile("s_waitcnt vmcnt(0) lgkmcnt(0)" ::: "memory");
    __builtin_amdgcn_s_barrier();
    asm volatile("" ::: "memory");
    u32 actmask = 0;
#pragma unroll
    for (int it = 0; it < 4; ++it) {
        const int e = (it * K1T + tid) * 4;
        if (e < n) {
            const u4 a = *(const u4*)(eS + e), b = *(const u4*)(eD + e);
            u32 m4 = 0;
#pragma unroll
            for (int j = 0; j < 4; ++j) {
                float ax, ay, az, bx, by, bz;
                get_pos<POS_LDS>(lds, pos, a[j], ax, ay, az);
                get_pos<POS_LDS>(lds, pos, b[j], bx, by, bz);
                const float rx = ax - bx, ry = ay - by, rz = az - bz;
                const float d2 = __builtin_fmaf(rz, rz, __builtin_fmaf(ry, ry, __builtin_fmaf(rx, rx, 1e-8f)));
                m4 |= (d2 >= CUTOFF * CUTOFF && d2 <= 3.0e38f) ? 0u : (1u << j);
            }
            if (n - e < 4) m4 &= (1u << (n - e)) - 1u;
            actmask |= m4 << (it * 4);
        }
    }
    {
        const int cntt = __popc(actmask);
        const int inc = wave_incl_scan(cntt);
        u32 qb = 0;
        if (lane == 63) qb = atomicAdd(&ocnt[1], (u32)inc);
        qb = __builtin_amdgcn_readlane(qb, 63) + (inc - cntt);
        u32 m = actmask;
        while (m) {
            const int bit = __builtin_ctz(m); m &= m - 1;
            queue[qb++] = (u16)((((bit >> 2) * K1T + tid) << 2) + (bit & 3));
        }
    }
    lds_barrier();
    const int qn = ocnt[1];
    u4* cl = cells + (size_t)f * P * CAPC;
    for (int j = tid; j < qn; j += K1T) {
        const int idx = queue[j];
        const u32 s = eS[idx], dd = eD[idx];
        const u32 p = dd / NB;
        const u32 rank = atomicAdd(&hist[p], 1u);
        float ax, ay, az, bx, by, bz;
        get_pos<POS_LDS>(lds, pos, s, ax, ay, az);
        get_pos<POS_LDS>(lds, pos, dd, bx, by, bz);
        const u4 r = {s | ((dd - p * NB) << 24), __float_as_uint(ax - bx), __float_as_uint(ay - by), __float_as_uint(az - bz)};
        if (rank < CAPC) cl[p * CAPC + rank] = r;
        else { const u32 o = atomicAdd(&ocnt[0], 1u); ovf[(size_t)f * chunk + o] = r; ovfp[(size_t)f * chunk + o] = p; }
    }
    lds_barrier();
    for (int p = tid; p < P; p += K1T) cnts[(size_t)f * P + p] = hist[p];
    if (tid == 0) ovfn[f] = ocnt[0];
}

__global__ __launch_bounds__(K1T) void k1_kernel(const u16* __restrict__ sv, const u16* __restrict__ pos, const int* __restrict__ src,
                                                 const int* __restrict__ dst, const u16* __restrict__ w1, const u16* __restrict__ w2,
                                                 char* __restrict__ tmain, char* __restrict__ tz, char* __restrict__ rv,
                                                 u4* __restrict__ cells, u32* __restrict__ cnts, u4* __restrict__ ovf, u32* __restrict__ ovfp, u32* __restrict__ ovfn,
                                                 int N, int E, int nfilt, int chunk, int P) {
    __shared__ __attribute__((aligned(1024))) char lds[LDS_BYTES];
    const int b = blockIdx.x, ntiles = (N + TM - 1) / TM;
    if (b >= ntiles) {
        if (N * 6 + 16 <= POS_BYTES) filter_role<true>(lds, b - ntiles, chunk, pos, src, dst, cells, cnts, ovf, ovfp, ovfn, N, E, P);
        else filter_role<false>(lds, b - ntiles, chunk, pos, src, dst, cells, cnts, ovf, ovfp, ovfn, N, E, P);
    } else {
        gemm_role(lds, b, sv, w1, w2, tmain, tz, rv, N);
    }
}

template <int I> __device__ __forceinline__ u32 qb(u32 x) {
    return __builtin_amdgcn_update_dpp(0u, x, I | (I << 2) | (I << 4) | (I << 6), 0xf, 0xf, true);
}
template <int I> __device__ __forceinline__ float qbf(u32 x) { return __uint_as_float(qb<I>(x)); }
__device__ __forceinline__ float fast_sin_rev(float rev) { return __builtin_amdgcn_sinf(__builtin_amdgcn_fractf(rev)); }
__device__ __forceinline__ float fast_cos_rev(float rev) { return __builtin_amdgcn_cosf(__builtin_amdgcn_fractf(rev)); }

struct Buf { u4 d[4]; u32 z[4]; u4 rb; u32 pn; };

__global__ __launch_bounds__(K2T, 4) void k2_kernel(const u16* __restrict__ sv, const u16* __restrict__ freqs, const u16* __restrict__ wr,
                                                    const u4* __restrict__ cells, const u32* __restrict__ cnts, const u4* __restrict__ ovf,
                                                    const u32* __restrict__ ovfp, const u32* __restrict__ ovfn,
                                                    const char* __restrict__ tmain, const char* __restrict__ tz, const char* __restrict__ rv,
                                                    u16* __restrict__ out, int N, int C, int chunk, int P) {
    __shared__ u4 recsA[TCAP + 1];
    __shared__ u4 recsB[TCAP + 1];
    __shared__ u32 keys[NB * CAPN];
    __shared__ u16 Lx[NB * CAPN];
    __shared__ u16 slotL[TCAP];
    __shared__ __attribute__((aligned(16))) int ncnt[(NB + 3) / 4 * 4];
    __shared__ unsigned char order[NB];
    __shared__ int Tsh, tetctr, slow, slow2, novl, ovl[8];
    __shared__ __attribute__((aligned(16))) u32 resL[K2W][2][256];
    const int tid = threadIdx.x, lane = tid & 63, w = __builtin_amdgcn_readfirstlane(tid >> 6);
    constexpr int NP = 8 / S;
    const int x = blockIdx.x & 7, j = blockIdx.x >> 3;
    const int slice = x % S, part = x / S;
    const int p = j * NP + part;
    if (p >= P) return;
    const int n0 = p * NB;
    const int grp = lane / G, gl = lane % G;
    const int ch = slice * (128 / S) + 2 * gl;
    auto rm = __builtin_amdgcn_make_buffer_rsrc((void*)(tmain + (size_t)slice * N * (G * 16)), 0, (u32)N * (G * 16), 0x00020000);
    auto rz = __builtin_amdgcn_make_buffer_rsrc((void*)(tz + (size_t)slice * N * (G * 4)), 0, (u32)N * (G * 4), 0x00020000);
    auto ro = __builtin_amdgcn_make_buffer_rsrc((void*)out, 0, (u32)N * 1024u, 0x00020000);

    constexpr int RPT = (64 * CAPC + K2T - 1) / K2T;
    const int nslots = C * CAPC;
    const u32 cstride = (u32)P * CAPC * 16;
    auto rc = __builtin_amdgcn_make_buffer_rsrc((void*)(cells + (size_t)p * CAPC), 0, (u32)C * cstride - (u32)p * CAPC * 16, 0x00020000);
    auto rn = __builtin_amdgcn_make_buffer_rsrc((void*)(cnts + p), 0, ((u32)C * P - p) * 4, 0x00020000);
    u4 raw[RPT]; u32 rcnt[RPT];
#pragma unroll
    for (int k = 0; k < RPT; ++k) {
        const u32 q = tid + k * K2T, c = q / CAPC;
        raw[k] = __builtin_amdgcn_raw_buffer_load_b128(rc, c * cstride + (q % CAPC) * 16, 0, 0);
        rcnt[k] = __builtin_amdgcn_raw_buffer_load_b32(rn, c * (u32)P * 4, 0, 0);
    }
    const u32 fq = freqs[lane & 3];
    __builtin_amdgcn_sched_barrier(0);
    if (tid < (NB + 3) / 4 * 4) ncnt[tid] = (tid < NB) ? 0 : -1;
    if (tid == 64) { recsA[TCAP] = u4{0, 0, 0, 0}; recsB[TCAP] = u4{OOB, 0, 0, 0}; Tsh = 0; tetctr = K2W; slow2 = 0; slow = (nslots > RPT * K2T) ? 1 : 0; novl = 0; }
    u32 wk[3][4];
#pragma unroll
    for (int t = 0; t < 3; ++t)
#pragma unroll
        for (int k = 0; k < 4; ++k) wk[t][k] = *(const u32*)(wr + k * 384 + t * 128 + ch);
    lds_barrier();
    auto push_raw = [&](const u4& r, bool valid) {
        const unsigned long long m = __ballot(valid);
        u32 ub = 0;
        if (lane == 0) ub = atomicAdd(&Tsh, (int)__popcll(m));
        const u32 u = __builtin_amdgcn_readfirstlane(ub) + mbcnt64(m);
        if (valid && u < TCAP) recsB[u] = r;
    };
#pragma unroll
    for (int k = 0; k < RPT; ++k) {
        const u32 q = tid + k * K2T;
        if (rcnt[k] > CAPC && (q % CAPC) == 0) { const int o = atomicAdd(&novl, 1); if (o < 8) ovl[o] = q / CAPC; else slow = 1; }
        push_raw(raw[k], (q % CAPC) < rcnt[k]);
    }
    lds_barrier();
    if (slow | novl) {
        if (slow) {
            lds_barrier();
            if (tid == 0) Tsh = TCAP + 1;
        } else {
            const int no_ = novl;
            for (int oi = 0; oi < no_; ++oi) {
                const int c = ovl[oi], no = ovfn[c];
                for (int i0 = 0; i0 < no; i0 += K2T) {
                    const int i = min(i0 + tid, no - 1);
                    const u32 pp = ovfp[(size_t)c * chunk + i];
                    const u4 r = ovf[(size_t)c * chunk + i];
                    push_raw(r, i0 + tid < no && pp == (u32)p);
                }
            }
        }
        lds_barrier();
    }
    float fr[4];
#pragma unroll
    for (int k = 0; k < 4; ++k) fr[k] = __uint_as_float(__builtin_amdgcn_readlane(fq, k) << 16) * 0.15915494309189535f;
    const int T = Tsh;
    for (int u = tid; u < min(T, TCAP); u += K2T) {
        const u4 r = recsB[u];
        const int dl = r[0] >> 24;
        const int sl = atomicAdd(&ncnt[dl], 1);
        const u32 key = (r[0] & 0xffffffu) * (G * 16);
        if (sl < CAPN) keys[dl * CAPN + sl] = key; else slow2 = 1;
        slotL[u] = (u16)((dl << 8) | (sl & 255));
        const float rx = __uint_as_float(r[1]), ry = __uint_as_float(r[2]), rz_ = __uint_as_float(r[3]);
        const float d = __builtin_amdgcn_sqrtf(__builtin_fmaf(rz_, rz_, __builtin_fmaf(ry, ry, __builtin_fmaf(rx, rx, 1e-8f))));
        const float inv = __builtin_amdgcn_rcpf(d);
        const float fc = (d < CUTOFF) ? 0.5f * fast_cos_rev(d * (0.5f / CUTOFF)) + 0.5f : 0.0f * d;
        const float sc = inv * fc;
        float gk[4];
#pragma unroll
        for (int q = 0; q < 4; ++q) gk[q] = fast_sin_rev(d * fr[q]) * sc;
        const u32 h01 = pack2(gk[0], gk[1]), h23 = pack2(gk[2], gk[3]);
        const u32 l01 = pack2(gk[0] - lo2f(h01), gk[1] - hi2f(h01)), l23 = pack2(gk[2] - lo2f(h23), gk[3] - hi2f(h23));
        recsA[u] = u4{h01, h23, l01, l23};
        recsB[u] = u4{key, __float_as_uint(rx * inv), __float_as_uint(ry * inv), __float_as_uint(rz_ * inv)};
    }
    s4 wb[3][2];
#pragma unroll
    for (int t = 0; t < 3; ++t) {
        u2 b0 = {__builtin_amdgcn_perm(wk[t][1], wk[t][0], 0x05040100), __builtin_amdgcn_perm(wk[t][3], wk[t][2], 0x05040100)};
        u2 b1 = {__builtin_amdgcn_perm(wk[t][1], wk[t][0], 0x07060302), __builtin_amdgcn_perm(wk[t][3], wk[t][2], 0x07060302)};
        wb[t][0] = __builtin_bit_cast(s4, b0);
        wb[t][1] = __builtin_bit_cast(s4, b1);
    }
    lds_barrier();
    if (T > TCAP || slow2) {
        float frs[4];
#pragma unroll
        for (int k = 0; k < 4; ++k) frs[k] = __uint_as_float((u32)freqs[k] << 16);
        for (int tet = w; tet < NB / S; tet += K2W) {
            const int nl = tet * S + grp, node = n0 + nl;
            float acc[4][2] = {};
            for (int c = 0; c < 2 * C; ++c) {
                const int cc = c < C ? c : c - C;
                const u32 cn = cnts[(size_t)cc * P + p];
                const int l = c < C ? (int)min(cn, (u32)CAPC) : (cn > CAPC ? (int)ovfn[cc] : 0);
                for (int base = 0; base < l; base += 64) {
                    u4 rr = {INV, 0, 0, 0};
                    bool ok = base + lane < l;
                    if (ok) {
                        if (c < C) rr = cells[((size_t)cc * P + p) * CAPC + base + lane];
                        else if (ovfp[(size_t)cc * chunk + base + lane] == (u32)p) rr = ovf[(size_t)cc * chunk + base + lane];
                        else ok = false;
                    }
                    const int dl = rr[0] >> 24;
                    unsigned long long m = __ballot(ok && (dl / S == tet));
                    while (m) {
                        const int i = __builtin_ctzll(m); m &= m - 1;
                        const u32 q0 = __builtin_amdgcn_readlane(rr[0], i);
                        const float rx = __uint_as_float(__builtin_amdgcn_readlane(rr[1], i)), ry = __uint_as_float(__builtin_amdgcn_readlane(rr[2], i)),
                                    rz_ = __uint_as_float(__builtin_amdgcn_readlane(rr[3], i));
                        const float d = sqrtf(rx * rx + ry * ry + rz_ * rz_ + 1e-8f), inv = 1.0f / d;
                        const float fc = (d < CUTOFF) ? 0.5f * (cosf(3.14159265358979323846f * d / CUTOFF) + 1.0f) : 0.0f * d;
                        float W[3][2] = {};
#pragma unroll 1
                        for (int k = 0; k < 4; ++k) {
                            const float fk = k == 0 ? frs[0] : (k == 1 ? frs[1] : (k == 2 ? frs[2] : frs[3]));
                            const float gk = sinf(d * fk) * inv * fc;
#pragma unroll
                            for (int t = 0; t < 3; ++t) {
                                const u32 wv = k == 0 ? wk[t][0] : (k == 1 ? wk[t][1] : (k == 2 ? wk[t][2] : wk[t][3]));
                                W[t][0] += gk * lo2f(wv); W[t][1] += gk * hi2f(wv);
                            }
                        }
                        if ((int)(q0 >> 24) == nl) {
                            const u32 off = ((q0 & 0xffffffu) * G + gl) * 4u;
                            const u4 dd = __builtin_amdgcn_raw_buffer_load_b128(rm, off * 4u, 0, 0);
                            const u32 zz = __builtin_amdgcn_raw_buffer_load_b32(rz, off, 0, 0);
                            acc[0][0] += W[0][0] * lo2f(dd[0]); acc[0][1] += W[0][1] * hi2f(dd[0]);
                            const float t0 = W[2][0] * lo2f(dd[1]), t1 = W[2][1] * hi2f(dd[1]);
                            acc[1][0] += W[1][0] * lo2f(dd[2]) + t0 * rx * inv; acc[1][1] += W[1][1] * hi2f(dd[2]) + t1 * rx * inv;
                            acc[2][0] += W[1][0] * lo2f(dd[3]) + t0 * ry * inv; acc[2][1] += W[1][1] * hi2f(dd[3]) + t1 * ry * inv;
                            acc[3][0] += W[1][0] * lo2f(zz) + t0 * rz_ * inv;   acc[3][1] += W[1][1] * hi2f(zz) + t1 * rz_ * inv;
                        }
                    }
                }
            }
            if (node < N) {
#pragma unroll
                for (int q = 0; q < 4; ++q) {
                    const u32 r = *(const u32*)((const char*)sv + (size_t)node * 1024 + q * 256 + ch * 2);
                    *(u32*)((char*)out + (size_t)node * 1024 + q * 256 + ch * 2) = pack2(lo2f(r) + acc[q][0], hi2f(r) + acc[q][1]);
                }
            }
        }
        return;
    }
    if (tid >= K2T - 128 && tid - (K2T - 128) < NB) {
        const int n = tid - (K2T - 128), c = ncnt[n];
        int rk = 0;
        for (int i = 0; i < NB; i += 4) {
            typedef int i4 __attribute__((ext_vector_type(4)));
            const i4 q = *(const i4*)(ncnt + i);
#pragma unroll
            for (int k = 0; k < 4; ++k) rk += (q[k] > c || (q[k] == c && i + k < n)) ? 1 : 0;
        }
        order[rk] = (unsigned char)n;
    }
    for (int u = tid; u < T; u += K2T) {
        const int ds = slotL[u], dl = ds >> 8, sl = ds & 255;
        const int c = ncnt[dl];
        const u32 key = recsB[u][0];
        int rank = 0;
        for (int i = 0; i < c; ++i) { u32 ki = keys[dl * CAPN + i]; rank += (ki < key || (ki == key && i < sl)) ? 1 : 0; }
        Lx[dl * CAPN + rank] = (u16)u;
    }
    lds_barrier();
    float as0 = 0, as1 = 0, ax0 = 0, ax1 = 0, ay0 = 0, ay1 = 0, az0 = 0, az1 = 0;
    const u32 gl16 = gl * 16;
    int i_tet = -1, i_b = 0, i_mm = 0, i_st = 0, i_c = 0, i_nl = 0, i_par = 0;
    auto advance = [&]() -> bool {
        i_b += 4;
        if (i_b < i_mm) return true;
        if (i_tet < 0) i_tet = w;
        else {
            int t = 0;
            if (lane == 0) t = atomicAdd(&tetctr, 1);
            i_tet = __builtin_amdgcn_readfirstlane(t);
        }
        if (i_tet >= NB / S) return false;
        const int nl = order[i_tet * S + grp];
        i_nl = nl; i_st = nl * CAPN; i_c = ncnt[nl];
        int mm = 1;
#pragma unroll
        for (int g2 = 0; g2 < S; ++g2) mm = max(mm, __builtin_amdgcn_readlane(i_c, g2 * G));
        i_mm = mm;
        i_b = 0;
        i_par ^= 1;
        {
            constexpr int CPR = G / 4;
            const int rem = lane % (4 * CPR);
            const int node = min(n0 + (int)order[i_tet * S + lane / (4 * CPR)], N - 1);
            const char* src_ = USE_RV ? rv + ((size_t)slice * N + node) * (16 * G) + rem * 16
                                      : (const char*)sv + (size_t)node * 1024 + (rem / CPR) * 256 + slice * (G * 4) + (rem % CPR) * 16;
            dma16_hidden(src_, (char*)resL[w][i_par]);
        }
        return true;
    };
#define ISSUE1(B, I) { \
        const u32 off_ = qb<I>(B.rb[0]) + gl16; \
        B.d[I] = __builtin_amdgcn_raw_buffer_load_b128(rm, off_, 0, 0); \
        B.z[I] = __builtin_amdgcn_raw_buffer_load_b32(rz, off_ >> 2, 0, 0); }
#define ISSUE(B, OK) { \
        const int k_ = i_b + (lane & 3); \
        int idx_ = Lx[min(i_st + k_, NB * CAPN - 1)]; \
        idx_ = ((OK) && k_ < i_c) ? idx_ : TCAP; \
        B.pn = (u32)idx_ | ((u32)i_nl << 16); \
        B.rb = recsB[idx_]; \
        ISSUE1(B, 0) ISSUE1(B, 1) ISSUE1(B, 2) ISSUE1(B, 3) \
        C_##B##_n = (OK) ? min(4, i_mm - i_b) : 0; C_##B##_par = i_par; }
#define OPQ(X) asm volatile("" : "+v"(X))
#define STEP(B, I) { \
        const float rx = qbf<I>(B.rb[1]), ry = qbf<I>(B.rb[2]), rz_ = qbf<I>(B.rb[3]); \
        const float t0 = Wr0[I] * lo2f(B.d[I][1]), t1 = Wr1[I] * hi2f(B.d[I][1]); \
        as0 = __builtin_fmaf(Ws0[I], lo2f(B.d[I][0]), as0); OPQ(as0); as1 = __builtin_fmaf(Ws1[I], hi2f(B.d[I][0]), as1); OPQ(as1); \
        ax0 = __builtin_fmaf(Wv0[I], lo2f(B.d[I][2]), ax0); OPQ(ax0); ax1 = __builtin_fmaf(Wv1[I], hi2f(B.d[I][2]), ax1); OPQ(ax1); \
        ay0 = __builtin_fmaf(Wv0[I], lo2f(B.d[I][3]), ay0); OPQ(ay0); ay1 = __builtin_fmaf(Wv1[I], hi2f(B.d[I][3]), ay1); OPQ(ay1); \
        az0 = __builtin_fmaf(Wv0[I], lo2f(B.z[I]), az0); OPQ(az0);    az1 = __builtin_fmaf(Wv1[I], hi2f(B.z[I]), az1); OPQ(az1); \
        ax0 = __builtin_fmaf(t0, rx, ax0); OPQ(ax0); ax1 = __builtin_fmaf(t1, rx, ax1); OPQ(ax1); \
        ay0 = __builtin_fmaf(t0, ry, ay0); OPQ(ay0); ay1 = __builtin_fmaf(t1, ry, ay1); OPQ(ay1); \
        az0 = __builtin_fmaf(t0, rz_, az0); OPQ(az0); az1 = __builtin_fmaf(t1, rz_, az1); OPQ(az1); }
#define WC(T, C2) __builtin_amdgcn_mfma_f32_4x4x4bf16_1k(ghi, wb[T][C2], __builtin_amdgcn_mfma_f32_4x4x4bf16_1k(glo, wb[T][C2], zf_, 0, 0, 0), 0, 0, 0)
#define COMPUTE(B) { \
        const f4 zf_ = {0, 0, 0, 0}; \
        const u4 ga_ = recsA[B.pn & 0xffffu]; \
        const u2 ghi_ = {ga_[0], ga_[1]}, glo_ = {ga_[2], ga_[3]}; \
        const s4 ghi = __builtin_bit_cast(s4, ghi_), glo = __builtin_bit_cast(s4, glo_); \
        const f4 Ws0 = WC(0, 0), Ws1 = WC(0, 1), Wv0 = WC(1, 0), Wv1 = WC(1, 1), Wr0 = WC(2, 0), Wr1 = WC(2, 1); \
        asm volatile("" :: "v"(B.d[0]), "v"(B.d[1]), "v"(B.d[2]), "v"(B.d[3]), "v"(B.z[0]), "v"(B.z[1]), "v"(B.z[2]), "v"(B.z[3])); \
        STEP(B, 0) \
        if (C_##B##_n > 1) STEP(B, 1) \
        if (C_##B##_n > 2) STEP(B, 2) \
        if (C_##B##_n > 3) STEP(B, 3) \
        if (C_##B##_fin) { \
            const int nl_ = B.pn >> 16, node_ = n0 + nl_; \
            { \
                const u32* rp = resL[w][C_##B##_par] + grp * (4 * G) + gl; \
                const u32 r0 = rp[0], r1 = rp[G], r2 = rp[2 * G], r3 = rp[3 * G]; \
                const u32 o = (node_ < N) ? (u32)node_ * 1024u + ch * 2 : OOB; \
                __builtin_amdgcn_raw_buffer_store_b32(pack2(lo2f(r0) + as0, hi2f(r0) + as1), ro, o, 0, OUT_AUX); \
                __builtin_amdgcn_raw_buffer_store_b32(pack2(lo2f(r1) + ax0, hi2f(r1) + ax1), ro, o, 256, OUT_AUX); \
                __builtin_amdgcn_raw_buffer_store_b32(pack2(lo2f(r2) + ay0, hi2f(r2) + ay1), ro, o, 512, OUT_AUX); \
                __builtin_amdgcn_raw_buffer_store_b32(pack2(lo2f(r3) + az0, hi2f(r3) + az1), ro, o, 768, OUT_AUX); \
            } \
            as0 = as1 = ax0 = ax1 = ay0 = ay1 = az0 = az1 = 0.f; \
        } }
    {
        Buf A, B;
        int C_A_n, C_B_n, C_A_par, C_B_par; bool C_A_fin, C_B_fin;
        (void)advance();
        ISSUE(A, true) C_A_fin = (i_b + 4 >= i_mm);
        bool m2;
        do {
            const bool m1 = advance();
            ISSUE(B, m1) C_B_fin = m1 && (i_b + 4 >= i_mm);
            COMPUTE(A)
            m2 = m1 && advance();
            ISSUE(A, m2) C_A_fin = m2 && (i_b + 4 >= i_mm);
            COMPUTE(B)
        } while (m2);
    }
}

extern "C" void kernel_launch(void* const* d_in, const int* in_sizes, int n_in, void* d_out, int out_size, void* d_ws, size_t ws_size,
                              hipStream_t stream) {
    const u16* sv = (const u16*)d_in[0];
    const u16* pos = (const u16*)d_in[1];
    const int* src = (const int*)d_in[2];
    const int* dst = (const int*)d_in[3];
    const u16* freqs = (const u16*)d_in[4];
    const u16* w1 = (const u16*)d_in[5];
    const u16* w2 = (const u16*)d_in[6];
    const u16* wr = (const u16*)d_in[7];
    const int N = in_sizes[0] / 512, E = in_sizes[2];
    const int P = (N + NB - 1) / NB;
    const int chmax = std::min(CHMAX, ((LDS_BYTES - POS_BYTES - 4 * P - 64) / 10) & ~3);
    int nfilt = NFILT_MIN;
    while ((((E + nfilt - 1) / nfilt + 3) & ~3) > chmax) ++nfilt;
    const int chunk = ((E + nfilt - 1) / nfilt + 3) & ~3;
    char* ws = (char*)d_ws;
    size_t off = 0;
    auto take = [&](size_t bytes) { char* p = ws + off; off += (bytes + 255) / 256 * 256; return p; };
    char* tmain = take((size_t)N * 1024);
    char* tz = take((size_t)N * 256);
    char* rv = take((size_t)N * 1024);
    u4* cells = (u4*)take((size_t)nfilt * P * CAPC * 16);
    u32* cnts = (u32*)take((size_t)nfilt * P * 4);
    u4* ovf = (u4*)take((size_t)nfilt * chunk * 16);
    u32* ovfp = (u32*)take((size_t)nfilt * chunk * 4);
    u32* ovfn = (u32*)take((size_t)nfilt * 4);
    const int ntiles = (N + TM - 1) / TM;
    k1_kernel<<<nfilt + ntiles, K1T, 0, stream>>>(sv, pos, src, dst, w1, w2, tmain, tz, rv, cells, cnts, ovf, ovfp, ovfn, N, E, nfilt, chunk, P);
    constexpr int NP = 8 / S;
    const int nj = (P + NP - 1) / NP;
    k2_kernel<<<8 * nj, K2T, 0, stream>>>(sv, freqs, wr, cells, cnts, ovf, ovfp, ovfn, tmain, tz, rv, (u16*)d_out, N, nfilt, chunk, P);
}
```
